```python
import torch

def _softmax(x, axis=-1):
    return torch.softmax(x, dim=axis)
def _gelu(x):
    return torch.nn.functional.gelu(x, approximate="tanh")
def _silu(x):
    return torch.nn.functional.silu(x)
def _sigmoid(x):
    return torch.sigmoid(x)
def _layernorm(x, axis=-1, eps=1e-5):
    m = x.mean(dim=axis, keepdim=True)
    v = x.var(dim=axis, keepdim=True, unbiased=False)
    return (x - m) / torch.sqrt(v + eps)
def _scatter_add(tgt, idx, src, axis=0):
    return tgt.clone().index_add_(axis, idx, src)
def _segment_sum(src, seg, n):
    out = torch.zeros((n,) + src.shape[1:], dtype=src.dtype)
    return out.index_add_(0, seg, src)
def _pad(x, pw):
    flat = tuple(v for pair in reversed(pw) for v in pair)
    return torch.nn.functional.pad(x, flat)

kernel_name = 'gated_mix__ssm_conv_s5__dense_activation_gelu__prompt'

def setup_inputs():

    _w = (lambda a: (lambda t: t.to(torch.bfloat16) if t.is_floating_point() else t)(torch.as_tensor(a)))
    def _A_setup():
        B, S, D = 1, 2048, 512
        P = D
        Lambda_re = -0.5 * (lambda _x: torch.exp(torch.as_tensor(_x)))((lambda *s: torch.randn(*s, dtype=torch.bfloat16) * 0.2)(P))
        Lambda_im = (lambda *s: torch.randn(*s, dtype=torch.bfloat16) * 0.8)(P) * 1.0
        B_re = (lambda *s: torch.randn(*s, dtype=torch.bfloat16) * 0.2)(P, D) * (D ** -0.5)
        B_im = (lambda *s: torch.randn(*s, dtype=torch.bfloat16) * 0.2)(P, D) * (D ** -0.5)
        C_re = (lambda *s: torch.randn(*s, dtype=torch.bfloat16) * 0.2)(D, P) * (P ** -0.5)
        C_im = (lambda *s: torch.randn(*s, dtype=torch.bfloat16) * 0.2)(D, P) * (P ** -0.5)
        log_dt = (lambda *s: torch.randn(*s, dtype=torch.bfloat16) * 0.2)(P) * 0.3 - 2.0
        Dskip = (lambda *s: torch.randn(*s, dtype=torch.bfloat16) * 0.05)(D)
        return {
            "x": _w((lambda *s: torch.randn(*s, dtype=torch.bfloat16) * 0.05)(B, S, D)),
            "Lambda_re": _w(Lambda_re),
            "Lambda_im": _w(Lambda_im),
            "B_re": _w(B_re),
            "B_im": _w(B_im),
            "C_re": _w(C_re),
            "C_im": _w(C_im),
            "log_dt": _w(log_dt),
            "Dskip": _w(Dskip),
        }
    _A_in = _A_setup()

    def _B_setup():
        B, S, D, F = 1, 2048, 512, 1792
        return {
            "x": _w((lambda *s: torch.randn(*s, dtype=torch.bfloat16) * 0.05)(B, S, D)),
            "w1": _w((lambda *s: torch.randn(*s, dtype=torch.bfloat16) * 3.2)(D, F) * (D ** -0.5)),
            "b1": _w((lambda *s: torch.randn(*s, dtype=torch.bfloat16) * 3.2)(F) * 0.01),
            "w2": _w((lambda *s: torch.randn(*s, dtype=torch.bfloat16) * 0.1)(F, D) * (F ** -0.5)),
            "b2": _w((lambda *s: torch.randn(*s, dtype=torch.bfloat16) * 0.2)(D) * 0.01),
        }
    _B_in = _B_setup()

    return {
        "A_x": _A_in["x"],
        "A_Lambda_re": _A_in["Lambda_re"],
        "A_Lambda_im": _A_in["Lambda_im"],
        "A_B_re": _A_in["B_re"],
        "A_B_im": _A_in["B_im"],
        "A_C_re": _A_in["C_re"],
        "A_C_im": _A_in["C_im"],
        "A_log_dt": _A_in["log_dt"],
        "A_Dskip": _A_in["Dskip"],
        "B_x": _B_in["x"],
        "B_w1": _B_in["w1"],
        "B_b1": _B_in["b1"],
        "B_w2": _B_in["w2"],
        "B_b2": _B_in["b2"],
        "gate_w": _w((lambda *s: torch.randn(*s, dtype=torch.bfloat16) * 0.05)(512) * 16.0),
    }

def reference(A_x, A_Lambda_re, A_Lambda_im, A_B_re, A_B_im, A_C_re, A_C_im, A_log_dt, A_Dskip, B_x, B_w1, B_b1, B_w2, B_b2, gate_w):
    def _A_ref(x, Lambda_re, Lambda_im, B_re, B_im, C_re, C_im, log_dt, Dskip):
        Bsz, L, H = x.shape
        Lam = Lambda_re + 1j * Lambda_im
        Bmat = B_re + 1j * B_im
        Cmat = C_re + 1j * C_im
        dt = torch.exp(log_dt)
        Lam_bar = torch.exp(dt * Lam)
        B_bar = ((Lam_bar - 1.0) / Lam)[:, None] * Bmat
        Bu = torch.einsum("bld,pd->blp", x + 0j, B_bar)
        h = torch.zeros_like(Bu[:, 0, :])
        hs = []
        for t in range(L):
            h = Lam_bar[None, :] * h + Bu[:, t, :]
            hs.append(h)
        hseq = torch.stack(hs, 1)
        y = 2.0 * torch.einsum("blp,dp->bld", hseq, Cmat).real + x * Dskip[None, None, :]
        return y

    def _B_ref(x, w1, b1, w2, b2):
        h = torch.einsum("bsd,df->bsf", x, w1) + b1
        h = _gelu(h)
        return torch.einsum("bsf,fd->bsd", h, w2) + b2

    x = A_x
    g = _sigmoid((x * gate_w).sum(-1, keepdims=True))
    a = _A_ref(x, A_Lambda_re, A_Lambda_im, A_B_re, A_B_im, A_C_re, A_C_im, A_log_dt, A_Dskip)
    b = _B_ref(x, B_w1, B_b1, B_w2, B_b2)
    return g * a + (1 - g) * b
```

```cpp
#include <hip/hip_runtime.h>
#include <hip/hip_bf16.h>

typedef __hip_bfloat16 bf16;
#define L_ 2048
#define D_ 512
#define P_ 512
#define F_ 1792

__device__ __forceinline__ float b2f(bf16 v) { return __bfloat162float(v); }
__device__ __forceinline__ float rbf(float v) { return __bfloat162float(__float2bfloat16(v)); }

__global__ void k_setup(const bf16* lre, const bf16* lim, const bf16* logdt, float4* prm) {
    int p = blockIdx.x * blockDim.x + threadIdx.x;
    if (p >= P_) return;
    float lr = b2f(lre[p]), li = b2f(lim[p]);
    float dt = rbf(expf(b2f(logdt[p])));
    float ar = dt * lr, ai = dt * li;
    float e = expf(ar);
    float s, c;
    sincosf(ai, &s, &c);
    float br = e * c, bi = e * s;
    float nr = br - 1.0f, ni = bi;
    float den = lr * lr + li * li;
    float cr = (nr * lr + ni * li) / den;
    float ci = (ni * lr - nr * li) / den;
    prm[p] = make_float4(br, bi, cr, ci);
}

__global__ void k_bu(const bf16* x, const bf16* Bre, const bf16* Bim, const float4* prm, float2* bu) {
    int p = blockIdx.x * blockDim.x + threadIdx.x;
    int t = blockIdx.y;
    float sr = 0, si = 0;
    for (int d = 0; d < D_; ++d) {
        float xv = b2f(x[t * D_ + d]);
        sr += xv * b2f(Bre[p * D_ + d]);
        si += xv * b2f(Bim[p * D_ + d]);
    }
    float4 q = prm[p];
    bu[t * P_ + p] = make_float2(q.z * sr - q.w * si, q.z * si + q.w * sr);
}

__global__ void k_scan(const float4* prm, float2* bu) {
    int p = blockIdx.x * blockDim.x + threadIdx.x;
    float4 q = prm[p];
    float hr = 0, hi = 0;
    for (int t = 0; t < L_; ++t) {
        float2 b = bu[t * P_ + p];
        float nr = q.x * hr - q.y * hi + b.x;
        float ni = q.x * hi + q.y * hr + b.y;
        hr = nr; hi = ni;
        bu[t * P_ + p] = make_float2(hr, hi);
    }
}

__device__ __forceinline__ float gelu_tanh(float v) {
    const float k0 = 0.7978845608028654f, k1 = 0.044715f;
    float inner = k0 * (v + k1 * v * v * v);
    return 0.5f * v * (1.0f + tanhf(inner));
}

__global__ void k_mlp1(const bf16* x, const bf16* w1, const bf16* b1, bf16* hm) {
    int f = blockIdx.x * blockDim.x + threadIdx.x;
    int t = blockIdx.y;
    float s = 0;
    for (int d = 0; d < D_; ++d) s += b2f(x[t * D_ + d]) * b2f(w1[d * F_ + f]);
    float v = rbf(rbf(s) + b2f(b1[f]));
    hm[t * F_ + f] = __float2bfloat16(gelu_tanh(v));
}

__global__ void k_final(const bf16* x, const float2* h, const bf16* Cre, const bf16* Cim, const bf16* Dsk,
                        const bf16* hm, const bf16* w2, const bf16* b2, const bf16* gw, float* out) {
    int d = blockIdx.x * blockDim.x + threadIdx.x;
    int t = blockIdx.y;
    float gs = 0;
    for (int k = 0; k < D_; ++k) gs += rbf(b2f(x[t * D_ + k]) * b2f(gw[k]));
    gs = rbf(gs);
    float g = rbf(1.0f / (1.0f + expf(-gs)));
    float s = 0;
    for (int p = 0; p < P_; ++p) {
        float2 hv = h[t * P_ + p];
        s += hv.x * b2f(Cre[d * P_ + p]) - hv.y * b2f(Cim[d * P_ + p]);
    }
    float a = 2.0f * s + rbf(b2f(x[t * D_ + d]) * b2f(Dsk[d]));
    float m = 0;
    for (int f = 0; f < F_; ++f) m += b2f(hm[t * F_ + f]) * b2f(w2[f * D_ + d]);
    float b = rbf(rbf(m) + b2f(b2[d]));
    out[t * D_ + d] = g * a + rbf(rbf(1.0f - g) * b);
}

extern "C" void kernel_launch(void* const* d_in, const int* in_sizes, int n_in,
                              void* d_out, int out_size, void* d_ws, size_t ws_size,
                              hipStream_t stream) {
    const bf16* x = (const bf16*)d_in[0];
    const bf16* lre = (const bf16*)d_in[1];
    const bf16* lim = (const bf16*)d_in[2];
    const bf16* Bre = (const bf16*)d_in[3];
    const bf16* Bim = (const bf16*)d_in[4];
    const bf16* Cre = (const bf16*)d_in[5];
    const bf16* Cim = (const bf16*)d_in[6];
    const bf16* logdt = (const bf16*)d_in[7];
    const bf16* Dsk = (const bf16*)d_in[8];
    const bf16* w1 = (const bf16*)d_in[10];
    const bf16* b1 = (const bf16*)d_in[11];
    const bf16* w2 = (const bf16*)d_in[12];
    const bf16* b2 = (const bf16*)d_in[13];
    const bf16* gw = (const bf16*)d_in[14];
    float* out = (float*)d_out;
    char* ws = (char*)d_ws;
    float4* prm = (float4*)ws;
    float2* bu = (float2*)(ws + 65536);
    bf16* hm = (bf16*)(ws + 65536 + (size_t)L_ * P_ * 8);
    k_setup<<<2, 256, 0, stream>>>(lre, lim, logdt, prm);
    k_bu<<<dim3(P_ / 256, L_), 256, 0, stream>>>(x, Bre, Bim, prm, bu);
    k_scan<<<P_ / 64, 64, 0, stream>>>(prm, bu);
    k_mlp1<<<dim3(F_ / 256, L_), 256, 0, stream>>>(x, w1, b1, hm);
    k_final<<<dim3(D_ / 256, L_), 256, 0, stream>>>(x, bu, Cre, Cim, Dsk, hm, w2, b2, gw, out);
}
```

```cpp
#include <hip/hip_runtime.h>
#include <hip/hip_bf16.h>
#include <stdint.h>

typedef __hip_bfloat16 bf16;
typedef __attribute__((ext_vector_type(8))) short bf16x8;
typedef __attribute__((ext_vector_type(4))) short s16x4;
typedef __attribute__((ext_vector_type(4))) float f32x4;
typedef __attribute__((ext_vector_type(2))) float f32x2;
typedef __attribute__((ext_vector_type(16))) float f32x16;
typedef __attribute__((ext_vector_type(4))) unsigned u32x4;
typedef __attribute__((ext_vector_type(2))) unsigned u32x2;

#define L_ 2048
#define D_ 512
#define P_ 512
#define F_ 1792

#define WS_HA   0ull
#define WS_CF   (4ull << 20)
#define WS_HM   (8ull << 20)
#define WS_W2T  (16ull << 20)
#define WS_E64  ((18ull << 20) + 4096)
#define WS_E32  ((18ull << 20) + 139264)
#define WS_G    ((18ull << 20) + 270336)
#define WS_LAM  ((18ull << 20) + 280576)

#define SB __builtin_amdgcn_sched_barrier(0)

__device__ __forceinline__ float bits2f(unsigned u) { return __uint_as_float(u); }
__device__ __forceinline__ float b2f(bf16 v) { return __bfloat162float(v); }
__device__ __forceinline__ unsigned pack2(float lo, float hi) {
    unsigned r;
    asm("v_cvt_pk_bf16_f32 %0, %1, %2" : "=v"(r) : "v"(lo), "v"(hi));
    return r;
}
__device__ __forceinline__ float rbf(float v) { return bits2f(pack2(v, v) << 16); }

__device__ __forceinline__ void glds16(const void* src, void* lds) {
    __builtin_amdgcn_global_load_lds((const unsigned*)src, (unsigned*)lds, 16, 0, 0);
}

__device__ __forceinline__ void ssm_params(const bf16* lre, const bf16* lim, const bf16* logdt, int p,
                                           float& br, float& bi, float& cr, float& ci) {
    float lr = b2f(lre[p]), li = b2f(lim[p]);
    float dt = rbf(expf(b2f(logdt[p])));
    float ar = dt * lr, ai = dt * li;
    float e = expf(ar);
    float s, c;
    sincosf(ai, &s, &c);
    br = e * c; bi = e * s;
    float nr = br - 1.0f, ni = bi;
    float den = lr * lr + li * li;
    cr = (nr * lr + ni * li) / den;
    ci = (ni * lr - nr * li) / den;
}
__device__ __forceinline__ void csq(float& r, float& i) { float a = r * r - i * i, b = 2.0f * r * i; r = a; i = b; }

__device__ __forceinline__ float gelu_tanh(float v) {
    const float c0 = -2.0f * 1.4426950408889634f * 0.7978845608028654f;
    const float c1 = c0 * 0.044715f;
    float t = __builtin_fmaf(c1, v * v, c0);
    float e = __builtin_amdgcn_exp2f(v * t);
    return v * __builtin_amdgcn_rcpf(1.0f + e);
}

__device__ __forceinline__ f32x2 gelu2(f32x2 v) {
    const float c0 = -2.0f * 1.4426950408889634f * 0.7978845608028654f;
    const float c1 = c0 * 0.044715f;
    f32x2 t = (v * v) * c1 + c0;
    f32x2 a2 = v * t;
    f32x2 d = (f32x2){__builtin_amdgcn_exp2f(a2[0]), __builtin_amdgcn_exp2f(a2[1])} + 1.0f;
    return v * (f32x2){__builtin_amdgcn_rcpf(d[0]), __builtin_amdgcn_rcpf(d[1])};
}

#define STG 49152
#define BOFS(buf) ((buf) * STG)

struct K1Args {
    const bf16* x; const bf16* lre; const bf16* lim; const bf16* Bre; const bf16* Bim; const bf16* logdt;
    const bf16* w1; const bf16* b1; const bf16* w2; const bf16* gw; const bf16* Cre; const bf16* Cim;
    bf16* cf; bf16* ha; bf16* hm; bf16* w2t; float2* e64; float2* e32; float* gate; float2* lam;
};

template <bool SSM>
__device__ __forceinline__ void k1_gemm(const K1Args& a, char* smem, int rb, int cb) {
    const int tid = threadIdx.x, wid = tid >> 6, lane = tid & 63;
    const int wm = wid & 3, wn = wid >> 2;
    const int row0 = rb * 256;
    const int h = lane >> 5, i32 = lane & 31;

    const bf16* asrc[4];
    const bf16* bsrc[2];
#pragma unroll
    for (int i = 0; i < 4; ++i) {
        int j = wid + 8 * i;
        int row = 8 * j + (lane >> 3);
        int ch = (lane & 7) ^ ((row >> 1) & 7);
        asrc[i] = a.x + (size_t)(row0 + row) * D_ + ch * 8;
    }
#pragma unroll
    for (int i = 0; i < 2; ++i) {
        int j = wid + 8 * i;
        if (SSM) {
            int n = 8 * j + (lane >> 3);
            int ch = (lane & 7) ^ ((n >> 1) & 7);
            int chan = cb * 64 + (n >> 6) * 32 + (n & 31);
            const bf16* base = ((n >> 5) & 1) ? a.Bim : a.Bre;
            bsrc[i] = base + (size_t)chan * D_ + ch * 8;
        } else {
            int k = 4 * j + (lane >> 4);
            int ch = (lane & 15) ^ (((k & 3) << 2) | ((k >> 2) & 3));
            bsrc[i] = a.w1 + (size_t)k * F_ + (cb - 8) * 128 + ch * 8;
        }
    }
    const int bstep = SSM ? 64 : 64 * F_;

    auto stage = [&](int kt, int buf) {
        char* base = smem + buf * STG;
#pragma unroll
        for (int i = 0; i < 4; ++i) glds16(asrc[i] + kt * 64, base + (wid + 8 * i) * 1024 + lane * 16);
#pragma unroll
        for (int i = 0; i < 2; ++i) glds16(bsrc[i] + (size_t)kt * bstep, base + 32768 + (wid + 8 * i) * 1024 + lane * 16);
    };

    const unsigned lds0 = (unsigned)(size_t)smem;
    unsigned aaddr[2][4];
#pragma unroll
    for (int m = 0; m < 2; ++m) {
        int row = 64 * wm + 32 * ((i32 >> 2) & 1) + 16 * m + (i32 & 3) + 4 * (i32 >> 3);
        int sw = (row >> 1) & 7;
#pragma unroll
        for (int ks = 0; ks < 4; ++ks) aaddr[m][ks] = lds0 + row * 128 + (((2 * ks + h) ^ sw) << 4);
    }
    unsigned baddr[2][4];
#pragma unroll
    for (int nn = 0; nn < 2; ++nn) {
        if (SSM) {
            int row = 64 * wn + 32 * nn + i32;
            int sw = (row >> 1) & 7;
#pragma unroll
            for (int ks = 0; ks < 4; ++ks) baddr[nn][ks] = lds0 + 32768 + row * 128 + (((2 * ks + h) ^ sw) << 4);
        } else {
            const int blk = (lane >> 4) & 1, q = (lane & 15) >> 2, p = lane & 3;
            const int c = 2 * wn + nn;
#pragma unroll
            for (int t = 0; t < 2; ++t) {
                int row = 8 * h + 4 * t + q;
                int ch = 4 * c + 2 * blk + (p >> 1);
                baddr[nn][t] = lds0 + 32768 + 256 * row + 16 * (ch ^ (((row & 3) << 2) | ((row >> 2) & 3))) + 8 * (p & 1);
            }
            baddr[nn][2] = baddr[nn][3] = 0;
        }
    }

    f32x16 acc[2][2];
#pragma unroll
    for (int m = 0; m < 2; ++m)
#pragma unroll
        for (int nn = 0; nn < 2; ++nn)
#pragma unroll
            for (int r = 0; r < 16; ++r) acc[m][nn][r] = 0.f;

    bf16x8 fa[2][2], fb[2][2];
#define RD_FRAG(s, buf, ks)                                                                                   \
    do {                                                                                                      \
        _Pragma("unroll") for (int m = 0; m < 2; ++m)                                                         \
            asm volatile("ds_read_b128 %0, %1 offset:%2" : "=v"(fa[s][m]) : "v"(aaddr[m][ks] + BOFS(buf)), "i"((buf) * STG - BOFS(buf))); \
        if (SSM) {                                                                                            \
            _Pragma("unroll") for (int nn = 0; nn < 2; ++nn)                                                  \
                asm volatile("ds_read_b128 %0, %1 offset:%2" : "=v"(fb[s][nn]) : "v"(baddr[nn][ks] + BOFS(buf)), "i"((buf) * STG - BOFS(buf))); \
        } else {                                                                                              \
            _Pragma("unroll") for (int nn = 0; nn < 2; ++nn) {                                                \
                s16x4 lo_, hi_;                                                                               \
                asm volatile("ds_read_b64_tr_b16 %0, %1 offset:%2" : "=v"(lo_) : "v"(baddr[nn][0] + BOFS(buf)), "i"((buf) * STG - BOFS(buf) + (ks) * 4096)); \
                asm volatile("ds_read_b64_tr_b16 %0, %1 offset:%2" : "=v"(hi_) : "v"(baddr[nn][1] + BOFS(buf)), "i"((buf) * STG - BOFS(buf) + (ks) * 4096)); \
                fb[s][nn] = __builtin_shufflevector(lo_, hi_, 0, 1, 2, 3, 4, 5, 6, 7);                        \
            }                                                                                                 \
        }                                                                                                     \
    } while (0)
#define WAIT_FRAG(s, N)                                                                                       \
    asm volatile("s_waitcnt lgkmcnt(" #N ")" : "+v"(fa[s][0]), "+v"(fa[s][1]), "+v"(fb[s][0]), "+v"(fb[s][1]))
#define MM_FRAG(s)                                                                                            \
    do {                                                                                                      \
        _Pragma("unroll") for (int m = 0; m < 2; ++m)                                                         \
            _Pragma("unroll") for (int nn = 0; nn < 2; ++nn) {                                                \
                if (SSM) acc[m][nn] = __builtin_amdgcn_mfma_f32_32x32x16_bf16(fa[s][m], fb[s][nn], acc[m][nn], 0, 0, 0); \
                else     acc[m][nn] = __builtin_amdgcn_mfma_f32_32x32x16_bf16(fb[s][nn], fa[s][m], acc[m][nn], 0, 0, 0); \
            }                                                                                                 \
    } while (0)

    stage(0, 0);
    stage(1, 1);
#pragma unroll
    for (int kt = 0; kt < 8; ++kt) {
        if (kt < 7) asm volatile("s_waitcnt vmcnt(6)" ::: "memory");
        else        asm volatile("s_waitcnt vmcnt(0)" ::: "memory");
        __builtin_amdgcn_s_barrier();
        const int buf = kt % 3;
        RD_FRAG(0, buf, 0);
        if (kt + 2 < 8) stage(kt + 2, (kt + 2) % 3);
        SB;
        RD_FRAG(1, buf, 1);
        if (SSM) { WAIT_FRAG(0, 4); } else { WAIT_FRAG(0, 6); }
        SB; MM_FRAG(0); SB;
        RD_FRAG(0, buf, 2);
        if (SSM) { WAIT_FRAG(1, 4); } else { WAIT_FRAG(1, 6); }
        SB; MM_FRAG(1); SB;
        RD_FRAG(1, buf, 3);
        if (SSM) { WAIT_FRAG(0, 4); } else { WAIT_FRAG(0, 6); }
        SB; MM_FRAG(0); SB;
        WAIT_FRAG(1, 0);
        SB; MM_FRAG(1); SB;
    }

    __builtin_amdgcn_s_barrier();
    if (SSM) {
        const int chan = cb * 64 + wn * 32 + i32;
        float lr, li, cr, ci;
        ssm_params(a.lre, a.lim, a.logdt, chan, lr, li, cr, ci);
        float hr = 0.f, hi = 0.f;
        const int tb = 64 * wm + 32 * h;
        const int c_re = 64 * wn + i32, c_im = c_re + 32;
#pragma unroll
        for (int m = 0; m < 2; ++m)
#pragma unroll
            for (int r = 0; r < 16; ++r) {
                float ur = acc[m][0][r], ui = acc[m][1][r];
                float br = cr * ur - ci * ui, bi = cr * ui + ci * ur;
                float nr = lr * hr - li * hi + br;
                float ni = lr * hi + li * hr + bi;
                hr = nr; hi = ni;
                unsigned pk = pack2(hr, -hi);
                const int t = tb + 16 * m + r;
                const int sw = (t >> 2) & 15;
                *(unsigned short*)(smem + t * 256 + (((c_re >> 3) ^ sw) << 4) + (c_re & 7) * 2) = (unsigned short)pk;
                *(unsigned short*)(smem + t * 256 + (((c_im >> 3) ^ sw) << 4) + (c_im & 7) * 2) = (unsigned short)(pk >> 16);
            }
        float e0r = __shfl(hr, i32), e0i = __shfl(hi, i32);
        const int chunk = (row0 >> 6) + wm;
        float pr = lr, pi = li;
#pragma unroll
        for (int s = 0; s < 5; ++s) csq(pr, pi);
        if (h == 0) {
            a.e32[chunk * P_ + chan] = make_float2(hr, hi);
        } else {
            a.e64[chunk * P_ + chan] = make_float2(hr + pr * e0r - pi * e0i, hi + pr * e0i + pi * e0r);
        }
    } else {
        const int n0 = (cb - 8) * 128;
        u32x2 bb[2][4];
#pragma unroll
        for (int nn = 0; nn < 2; ++nn)
#pragma unroll
            for (int q = 0; q < 4; ++q) bb[nn][q] = *(const u32x2*)(a.b1 + n0 + 64 * wn + 32 * nn + 8 * q + 4 * h);
#pragma unroll
        for (int nn = 0; nn < 2; ++nn) {
#pragma unroll
            for (int q = 0; q < 4; ++q) {
                const int nc = 8 * wn + 4 * nn + q;
                float bv[4] = {bits2f(bb[nn][q][0] << 16), bits2f(bb[nn][q][0] & 0xffff0000u),
                               bits2f(bb[nn][q][1] << 16), bits2f(bb[nn][q][1] & 0xffff0000u)};
#pragma unroll
                for (int m = 0; m < 2; ++m) {
                    const int t = 64 * wm + 32 * ((i32 >> 2) & 1) + 16 * m + (i32 & 3) + 4 * (i32 >> 3);
                    f32x2 g0 = gelu2((f32x2){acc[m][nn][4 * q], acc[m][nn][4 * q + 1]} + (f32x2){bv[0], bv[1]});
                    f32x2 g1 = gelu2((f32x2){acc[m][nn][4 * q + 2], acc[m][nn][4 * q + 3]} + (f32x2){bv[2], bv[3]});
                    u32x2 o; o[0] = pack2(g0[0], g0[1]); o[1] = pack2(g1[0], g1[1]);
                    *(u32x2*)(smem + t * 256 + ((nc ^ ((t >> 2) & 15)) << 4) + 8 * h) = o;
                }
            }
        }
    }
    __syncthreads();
    {
        const int i16 = lane & 15, g4 = lane >> 4;
#pragma unroll
        for (int it = 0; it < 8; ++it) {
            const int f = it * 8 + wid;
            const int rtl = f >> 2, ksl = f & 3;
            const int t = 64 * (rtl >> 2) + 4 * i16 + (rtl & 3);
            u32x4 v = *(const u32x4*)(smem + t * 256 + (((4 * ksl + g4) ^ ((t >> 2) & 15)) << 4));
            const int rt = rb * 16 + rtl;
            if (SSM) {
                const int ks = (ksl & 1) * 16 + 2 * cb + (ksl >> 1);
                *(u32x4*)((char*)a.ha + ((size_t)(rt * 32 + ks) * 64 + lane) * 16) = v;
            } else {
                const int ks = (cb - 8) * 4 + ksl;
                *(u32x4*)((char*)a.hm + ((size_t)(rt * 56 + ks) * 64 + lane) * 16) = v;
            }
        }
    }
}

__global__ __launch_bounds__(512, 2) void k1_kernel(K1Args a) {
    __shared__ __attribute__((aligned(16))) char smem[3 * STG];
    const int bid = blockIdx.x;
    const int tid = threadIdx.x, wid = tid >> 6, lane = tid & 63;
    if (bid < 192) {
        const int xcd = bid & 7, idx = bid >> 3;
        const int cg = xcd >> 1, ci = idx >> 2;
        const int rb = (xcd & 1) * 4 + (idx & 3);
        const int cb = (cg < 2 ? cg * 6 : 2 + cg * 5) + ci;
        if (cg >= 2 && ci == 5) return;
        if (cb < 8) k1_gemm<true>(a, smem, rb, cb);
        else        k1_gemm<false>(a, smem, rb, cb);
        return;
    }
    const int sid = bid - 192;
    if (sid < 28) {
        const int k0 = sid * 64;
        unsigned short* sm = (unsigned short*)smem;
#pragma unroll
        for (int it = 0; it < 8; ++it) {
            int idx = it * 512 + tid;
            int r = idx >> 6, c = idx & 63;
            u32x4 v = *(const u32x4*)(a.w2 + (size_t)(k0 + r) * D_ + c * 8);
            *(u32x4*)(sm + r * 520 + c * 8) = v;
        }
        __syncthreads();
#pragma unroll
        for (int it = 0; it < 8; ++it) {
            const int f = it * 8 + wid;
            const int nt = f >> 1, ksl = f & 1;
            const int n = 16 * nt + (lane & 15), kb = 32 * ksl + 8 * (lane >> 4);
            unsigned short v[8];
#pragma unroll
            for (int e = 0; e < 8; ++e) v[e] = sm[(kb + e) * 520 + n];
            u32x4 o;
#pragma unroll
            for (int e = 0; e < 4; ++e) o[e] = (unsigned)v[2 * e] | ((unsigned)v[2 * e + 1] << 16);
            *(u32x4*)((char*)a.w2t + ((size_t)(nt * 56 + (k0 >> 5) + ksl) * 64 + lane) * 16) = o;
        }
        return;
    }
    if (sid >= 61) {
#pragma unroll
        for (int it = 0; it < 16; ++it) {
            const int f = (sid - 61) * 128 + it * 8 + wid;
            const int nt = f >> 5, ks = f & 31;
            const bf16* src = (ks < 16 ? a.Cre : a.Cim) + (size_t)(16 * nt + (lane & 15)) * P_ + 32 * (ks & 15) + 8 * (lane >> 4);
            *(u32x4*)((char*)a.cf + ((size_t)f * 64 + lane) * 16) = *(const u32x4*)src;
        }
        return;
    }
    if (sid < 60) {
        const int r0 = (sid - 28) * 64 + wid * 8;
        u32x4 gv = *(const u32x4*)(a.gw + lane * 8);
        u32x4 xv[8];
#pragma unroll
        for (int r = 0; r < 8; ++r) xv[r] = *(const u32x4*)(a.x + (size_t)(r0 + r) * D_ + lane * 8);
        float gwf[8];
#pragma unroll
        for (int e = 0; e < 4; ++e) { gwf[2 * e] = bits2f(gv[e] << 16); gwf[2 * e + 1] = bits2f(gv[e] & 0xffff0000u); }
        float mine = 0.f;
#pragma unroll
        for (int r = 0; r < 8; ++r) {
            float s = 0.f;
#pragma unroll
            for (int e = 0; e < 4; ++e) {
                s += rbf(bits2f(xv[r][e] << 16) * gwf[2 * e]);
                s += rbf(bits2f(xv[r][e] & 0xffff0000u) * gwf[2 * e + 1]);
            }
#pragma unroll
            for (int o = 32; o > 0; o >>= 1) s += __shfl_xor(s, o);
            if (lane == r) mine = s;
        }
        if (lane < 8) {
            float gs = rbf(mine);
            a.gate[r0 + lane] = rbf(1.0f / (1.0f + expf(-gs)));
        }
        return;
    }
    {
        const int p = tid;
        float lr, li, t0, t1;
        ssm_params(a.lre, a.lim, a.logdt, p, lr, li, t0, t1);
        a.lam[p] = make_float2(lr, li);
        csq(lr, li); csq(lr, li);
        a.lam[512 + p] = make_float2(lr, li);
        csq(lr, li); csq(lr, li); csq(lr, li);
        a.lam[1024 + p] = make_float2(lr, li);
        csq(lr, li);
        a.lam[1536 + p] = make_float2(lr, li);
        a.e64[p - P_] = make_float2(0.f, 0.f);
    }
}

struct K2Args {
    const bf16* x; const bf16* cf;
    const bf16* Dsk; const bf16* b2;
    const bf16* ha; const bf16* hm; const bf16* w2t; const float2* e64; const float2* e32; const float* gate;
    const float2* lam;
    float* out;
};

#define ZS_STRIDE 4112
#define LAM_OFF (16 * ZS_STRIDE)

#define K2_SETUP_LOADS()                                                                             \
    f32x2 ev[31];                                                                                    \
    {                                                                                                \
        const f32x2* ep = (const f32x2*)a.e64 + tid;                                                 \
        _Pragma("unroll") for (int i = 0; i < 31; ++i) {                                             \
            int j = rb - 1 - i; j = j < -1 ? -1 : j;                                                 \
            ev[i] = ep[j * P_];                                                                      \
        }                                                                                            \
    }                                                                                                \
    const f32x2 e32v = *((const f32x2*)a.e32 + (size_t)rb * P_ + tid);                               \
    const f32x2 l1 = *((const f32x2*)a.lam + tid);                                                   \
    const f32x2 l4 = *((const f32x2*)a.lam + 512 + tid);                                             \
    const f32x2 l32 = *((const f32x2*)a.lam + 1024 + tid);                                           \
    const f32x2 l64 = *((const f32x2*)a.lam + 1536 + tid);
#define K2_SETUP_COMPUTE()                                                                           \
    {                                                                                                \
        float Cr = 0.f, Ci = 0.f;                                                                    \
        _Pragma("unroll") for (int i = 30; i >= 0; --i) {                                            \
            float nr = l64[0] * Cr - l64[1] * Ci + ev[i][0];                                         \
            float ni = l64[0] * Ci + l64[1] * Cr + ev[i][1];                                         \
            Cr = nr; Ci = ni;                                                                        \
        }                                                                                            \
        const float c1r = e32v[0] + l32[0] * Cr - l32[1] * Ci;                                       \
        const float c1i = e32v[1] + l32[0] * Ci + l32[1] * Cr;                                       \
        _Pragma("unroll") for (int sub = 0; sub < 2; ++sub) {                                        \
            const float cr_ = sub ? c1r : Cr, ci_ = sub ? c1i : Ci;                                  \
            float zr = cr_ * l1[0] - ci_ * l1[1];                                                    \
            float zi = cr_ * l1[1] + ci_ * l1[0];                                                    \
            _Pragma("unroll") for (int ii = 0; ii < 8; ++ii) {                                       \
                *(f32x2*)(smem + (sub * 8 + ii) * ZS_STRIDE + tid * 8) = (f32x2){zr, zi};            \
                float nr = zr * l4[0] - zi * l4[1];                                                  \
                float ni = zr * l4[1] + zi * l4[0];                                                  \
                zr = nr; zi = ni;                                                                    \
            }                                                                                        \
        }                                                                                            \
        *(f32x2*)(smem + LAM_OFF + tid * 8) = l1;                                                    \
    }                                                                                                \
    asm volatile("s_waitcnt lgkmcnt(0)" ::: "memory");                                               \
    __builtin_amdgcn_s_barrier();

__global__ __launch_bounds__(512, 2) void k2_kernel(K2Args a) {
    __shared__ __attribute__((aligned(16))) char smem[131072];
    const int bid = blockIdx.x;
    const int tid = threadIdx.x, wid = tid >> 6, lane = tid & 63;
    const int idx = bid >> 3;
    const int rb = (bid & 7) * 4 + (idx >> 3), cbk = idx & 7;
    const int row0 = rb * 64, col0 = cbk * 64;
    const int i16 = lane & 15, g4 = lane >> 4;
    const int widu = __builtin_amdgcn_readfirstlane(wid);
    const int voff = lane * 16;

    f32x4 acc[4][4];
#pragma unroll
    for (int m = 0; m < 4; ++m)
#pragma unroll
        for (int n = 0; n < 4; ++n) acc[m][n] = (f32x4){0.f, 0.f, 0.f, 0.f};

    if (widu < 6) {
        const bool mlp = widu < 4;
        const __amdgpu_buffer_rsrc_t ra = __builtin_amdgcn_make_buffer_rsrc(
            mlp ? (void*)a.hm : (void*)a.ha, 0, mlp ? 128 * 56 * 1024 : 128 * 32 * 1024, 0x00020000);
        const __amdgpu_buffer_rsrc_t rbs = __builtin_amdgcn_make_buffer_rsrc(
            mlp ? (void*)a.w2t : (void*)a.cf, 0, mlp ? 32 * 56 * 1024 : 32 * 32 * 1024, 0x00020000);
        const int nks = mlp ? 56 : 32;
        const int s0 = mlp ? widu * 14 : (widu - 4) * 16;
        const int ns = mlp ? 14 : 16;
        const int abase = (rb * 4 * nks + s0) * 1024, bbase = (cbk * 4 * nks + s0) * 1024;
        const int tstr = nks * 1024;
        bf16x8 A[4][4], B[4][4];
#define K2LD(bi, s)                                                                                  \
        do {                                                                                         \
            _Pragma("unroll") for (int m = 0; m < 4; ++m)                                            \
                A[bi][m] = __builtin_bit_cast(bf16x8, __builtin_amdgcn_raw_buffer_load_b128(ra, voff, abase + m * tstr + (s) * 1024, 0)); \
            _Pragma("unroll") for (int n = 0; n < 4; ++n)                                            \
                B[bi][n] = __builtin_bit_cast(bf16x8, __builtin_amdgcn_raw_buffer_load_b128(rbs, voff, bbase + n * tstr + (s) * 1024, 0)); \
        } while (0)
#define K2MM(bi)                                                                                     \
        do {                                                                                         \
            _Pragma("unroll") for (int m = 0; m < 4; ++m)                                            \
                _Pragma("unroll") for (int n = 0; n < 4; ++n)                                        \
                    acc[m][n] = __builtin_amdgcn_mfma_f32_16x16x32_bf16(A[bi][m], B[bi][n], acc[m][n], 0, 0, 0); \
        } while (0)
        K2_SETUP_LOADS();
        SB;
        K2LD(0, 0); K2LD(1, 1); K2LD(2, 2);
        SB;
        K2_SETUP_COMPUTE();
        SB;
#pragma unroll
        for (int s = 0; s < 12; ++s) {
            SB;
            K2LD((s + 3) % 4, s + 3);
            SB;
            K2MM(s % 4);
        }
        SB; if (!mlp) K2LD(3, 15);
        SB; K2MM(0);
        SB; K2MM(1);
        if (!mlp) { SB; K2MM(2); SB; K2MM(3); }
        SB;
    } else {
        const int wz = widu - 6;
        const __amdgpu_buffer_rsrc_t rbs = __builtin_amdgcn_make_buffer_rsrc((void*)a.cf, 0, 32 * 32 * 1024, 0x00020000);
        const int bbase = (cbk * 4 * 32 + wz * 8) * 1024;
        bf16x8 Br[2][4], Bi[2][4];
#define ZLD(bi, sl)                                                                                  \
        do {                                                                                         \
            _Pragma("unroll") for (int n = 0; n < 4; ++n) {                                          \
                Br[bi][n] = __builtin_bit_cast(bf16x8, __builtin_amdgcn_raw_buffer_load_b128(rbs, voff, bbase + (n * 32 + (sl)) * 1024, 0)); \
                Bi[bi][n] = __builtin_bit_cast(bf16x8, __builtin_amdgcn_raw_buffer_load_b128(rbs, voff, bbase + (n * 32 + 16 + (sl)) * 1024, 0)); \
            }                                                                                        \
        } while (0)
        K2_SETUP_LOADS();
        SB;
        ZLD(0, 0);
        SB;
        K2_SETUP_COMPUTE();
        SB;
        const char* zt = smem + i16 * ZS_STRIDE + (256 * wz + 8 * g4) * 8;
        const char* lt = smem + LAM_OFF + (256 * wz + 8 * g4) * 8;
#pragma unroll
        for (int sl = 0; sl < 8; ++sl) {
            const int cur = sl & 1;
            SB;
            if (sl + 1 < 8) ZLD(cur ^ 1, sl + 1);
            SB;
            f32x4 z[4], lm[4];
#pragma unroll
            for (int q = 0; q < 4; ++q) {
                z[q] = *(const f32x4*)(zt + sl * 256 + q * 16);
                lm[q] = *(const f32x4*)(lt + sl * 256 + q * 16);
            }
#pragma unroll
            for (int m = 0; m < 4; ++m) {
                u32x4 zr, zi;
#pragma unroll
                for (int q = 0; q < 4; ++q) {
                    zr[q] = pack2(z[q][0], z[q][2]);
                    zi[q] = pack2(-z[q][1], -z[q][3]);
                    if (m < 3) {
                        float nr0 = z[q][0] * lm[q][0] - z[q][1] * lm[q][1];
                        float ni0 = z[q][0] * lm[q][1] + z[q][1] * lm[q][0];
                        float nr1 = z[q][2] * lm[q][2] - z[q][3] * lm[q][3];
                        float ni1 = z[q][2] * lm[q][3] + z[q][3] * lm[q][2];
                        z[q] = (f32x4){nr0, ni0, nr1, ni1};
                    }
                }
                bf16x8 Zr = __builtin_bit_cast(bf16x8, zr), Zi = __builtin_bit_cast(bf16x8, zi);
#pragma unroll
                for (int n = 0; n < 4; ++n) {
                    acc[m][n] = __builtin_amdgcn_mfma_f32_16x16x32_bf16(Zr, Br[cur][n], acc[m][n], 0, 0, 0);
                    acc[m][n] = __builtin_amdgcn_mfma_f32_16x16x32_bf16(Zi, Bi[cur][n], acc[m][n], 0, 0, 0);
                }
            }
        }
        SB;
    }
    float gv[2][4], xv[2][4], dsk[2], bb[2];
#pragma unroll
    for (int f = 0; f < 2; ++f) {
        const int fr = wid * 2 + f;
        const int m = fr >> 2, n = fr & 3;
        const int d = col0 + 16 * n + i16;
        dsk[f] = b2f(a.Dsk[d]); bb[f] = b2f(a.b2[d]);
#pragma unroll
        for (int r = 0; r < 4; ++r) {
            const int t = row0 + 16 * g4 + 4 * r + m;
            gv[f][r] = a.gate[t];
            xv[f][r] = b2f(a.x[(size_t)t * D_ + d]);
        }
    }
    __syncthreads();
#pragma unroll
    for (int m = 0; m < 4; ++m)
#pragma unroll
        for (int n = 0; n < 4; ++n)
            *(f32x4*)(smem + ((wid * 16 + m * 4 + n) * 64 + lane) * 16) = acc[m][n];
    __syncthreads();
#pragma unroll
    for (int f = 0; f < 2; ++f) {
        const int fr = wid * 2 + f;
        const int m = fr >> 2, n = fr & 3;
        f32x4 sm_ = (f32x4){0.f, 0.f, 0.f, 0.f}, ss = (f32x4){0.f, 0.f, 0.f, 0.f};
#pragma unroll
        for (int w = 0; w < 4; ++w) {
            sm_ += *(const f32x4*)(smem + ((w * 16 + fr) * 64 + lane) * 16);
            ss += *(const f32x4*)(smem + (((w + 4) * 16 + fr) * 64 + lane) * 16);
        }
        const int d = col0 + 16 * n + i16;
#pragma unroll
        for (int r = 0; r < 4; ++r) {
            const int t = row0 + 16 * g4 + 4 * r + m;
            const float g = gv[f][r];
            const float av = 2.0f * ss[r] + rbf(xv[f][r] * dsk[f]);
            const float bv = rbf(rbf(sm_[r]) + bb[f]);
            a.out[(size_t)t * D_ + d] = g * av + rbf(rbf(1.0f - g) * bv);
        }
    }
}

extern "C" void kernel_launch(void* const* d_in, const int* in_sizes, int n_in,
                              void* d_out, int out_size, void* d_ws, size_t ws_size,
                              hipStream_t stream) {
    char* ws = (char*)d_ws;
    K1Args a1;
    a1.x = (const bf16*)d_in[0]; a1.lre = (const bf16*)d_in[1]; a1.lim = (const bf16*)d_in[2];
    a1.Bre = (const bf16*)d_in[3]; a1.Bim = (const bf16*)d_in[4]; a1.logdt = (const bf16*)d_in[7];
    a1.w1 = (const bf16*)d_in[10]; a1.b1 = (const bf16*)d_in[11]; a1.w2 = (const bf16*)d_in[12];
    a1.gw = (const bf16*)d_in[14];
    a1.Cre = (const bf16*)d_in[5]; a1.Cim = (const bf16*)d_in[6]; a1.cf = (bf16*)(ws + WS_CF);
    a1.ha = (bf16*)(ws + WS_HA); a1.hm = (bf16*)(ws + WS_HM); a1.w2t = (bf16*)(ws + WS_W2T);
    a1.e64 = (float2*)(ws + WS_E64); a1.e32 = (float2*)(ws + WS_E32); a1.gate = (float*)(ws + WS_G);
    a1.lam = (float2*)(ws + WS_LAM);
    K2Args a2;
    a2.x = a1.x;
    a2.cf = a1.cf; a2.Dsk = (const bf16*)d_in[8];
    a2.b2 = (const bf16*)d_in[13];
    a2.ha = a1.ha; a2.hm = a1.hm; a2.w2t = a1.w2t; a2.e64 = a1.e64; a2.e32 = a1.e32; a2.gate = a1.gate;
    a2.lam = a1.lam;
    a2.out = (float*)d_out;
    k1_kernel<<<261, 512, 0, stream>>>(a1);
    k2_kernel<<<256, 512, 0, stream>>>(a2);
}
```

```cpp
#include <hip/hip_runtime.h>
#include <hip/hip_bf16.h>
#include <stdint.h>

typedef __hip_bfloat16 bf16;
typedef __attribute__((ext_vector_type(8))) short bf16x8;
typedef __attribute__((ext_vector_type(4))) short s16x4;
typedef __attribute__((ext_vector_type(4))) float f32x4;
typedef __attribute__((ext_vector_type(2))) float f32x2;
typedef __attribute__((ext_vector_type(16))) float f32x16;
typedef __attribute__((ext_vector_type(4))) unsigned u32x4;
typedef __attribute__((ext_vector_type(2))) unsigned u32x2;

#define L_ 2048
#define D_ 512
#define P_ 512
#define F_ 1792

#define WS_HA   0ull
#define WS_CF   (4ull << 20)
#define WS_HM   (8ull << 20)
#define WS_W2T  (16ull << 20)
#define WS_E64  ((18ull << 20) + 4096)
#define WS_E32  ((18ull << 20) + 139264)
#define WS_G    ((18ull << 20) + 270336)
#define WS_LAM  ((18ull << 20) + 280576)
#define WS_E256 ((18ull << 20) + 307200)

#define SB __builtin_amdgcn_sched_barrier(0)

__device__ __forceinline__ float bits2f(unsigned u) { return __uint_as_float(u); }
__device__ __forceinline__ float b2f(bf16 v) { return __bfloat162float(v); }
typedef __attribute__((ext_vector_type(2))) __bf16 nbf16x2;
__device__ __forceinline__ unsigned pack2(float lo, float hi) {
    nbf16x2 v = {(__bf16)lo, (__bf16)hi};
    return __builtin_bit_cast(unsigned, v);
}
__device__ __forceinline__ float rbf(float v) { return bits2f(pack2(v, v) << 16); }

__device__ __forceinline__ void glds16(const void* src, void* lds) {
    __builtin_amdgcn_global_load_lds((const unsigned*)src, (unsigned*)lds, 16, 0, 0);
}

__device__ __forceinline__ void ssm_params(const bf16* lre, const bf16* lim, const bf16* logdt, int p,
                                           float& br, float& bi, float& cr, float& ci) {
    float lr = b2f(lre[p]), li = b2f(lim[p]);
    float dt = rbf(expf(b2f(logdt[p])));
    float ar = dt * lr, ai = dt * li;
    float e = expf(ar);
    float s, c;
    sincosf(ai, &s, &c);
    br = e * c; bi = e * s;
    float nr = br - 1.0f, ni = bi;
    float den = lr * lr + li * li;
    cr = (nr * lr + ni * li) / den;
    ci = (ni * lr - nr * li) / den;
}
__device__ __forceinline__ void csq(float& r, float& i) { float a = r * r - i * i, b = 2.0f * r * i; r = a; i = b; }

__device__ __forceinline__ float gelu_tanh(float v) {
    const float c0 = -2.0f * 1.4426950408889634f * 0.7978845608028654f;
    const float c1 = c0 * 0.044715f;
    float t = __builtin_fmaf(c1, v * v, c0);
    float e = __builtin_amdgcn_exp2f(v * t);
    return v * __builtin_amdgcn_rcpf(1.0f + e);
}

__device__ __forceinline__ f32x2 gelu2(f32x2 v) {
    const float c0 = -2.0f * 1.4426950408889634f * 0.7978845608028654f;
    const float c1 = c0 * 0.044715f;
    f32x2 t = (v * v) * c1 + c0;
    f32x2 a2 = v * t;
    f32x2 d = (f32x2){__builtin_amdgcn_exp2f(a2[0]), __builtin_amdgcn_exp2f(a2[1])} + 1.0f;
    return v * (f32x2){__builtin_amdgcn_rcpf(d[0]), __builtin_amdgcn_rcpf(d[1])};
}

#define STG 49152
#define BOFS(buf) ((buf) * STG)

struct K1Args {
    const bf16* x; const bf16* lre; const bf16* lim; const bf16* Bre; const bf16* Bim; const bf16* logdt;
    const bf16* w1; const bf16* b1; const bf16* w2; const bf16* gw; const bf16* Cre; const bf16* Cim;
    bf16* cf; bf16* ha; bf16* hm; bf16* w2t; float2* e64; float2* e32; float* gate; float2* lam; float2* e256;
};

template <bool SSM>
__device__ __forceinline__ void k1_gemm(const K1Args& a, char* smem, int rb, int cb) {
    const int tid = threadIdx.x, wid = tid >> 6, lane = tid & 63;
    const int wm = wid & 3, wn = wid >> 2;
    const int row0 = rb * 256;
    const int h = lane >> 5, i32 = lane & 31;

    const bf16* asrc[4];
    const bf16* bsrc[2];
#pragma unroll
    for (int i = 0; i < 4; ++i) {
        int j = wid + 8 * i;
        int row = 8 * j + (lane >> 3);
        int ch = (lane & 7) ^ ((row >> 1) & 7);
        asrc[i] = a.x + (size_t)(row0 + row) * D_ + ch * 8;
    }
#pragma unroll
    for (int i = 0; i < 2; ++i) {
        int j = wid + 8 * i;
        if (SSM) {
            int n = 8 * j + (lane >> 3);
            int ch = (lane & 7) ^ ((n >> 1) & 7);
            int chan = cb * 64 + (n >> 6) * 32 + (n & 31);
            const bf16* base = ((n >> 5) & 1) ? a.Bim : a.Bre;
            bsrc[i] = base + (size_t)chan * D_ + ch * 8;
        } else {
            int k = 4 * j + (lane >> 4);
            int ch = (lane & 15) ^ (((k & 3) << 2) | ((k >> 2) & 3));
            bsrc[i] = a.w1 + (size_t)k * F_ + (cb - 8) * 128 + ch * 8;
        }
    }
    const int bstep = SSM ? 64 : 64 * F_;

    auto stage = [&](int kt, int buf) {
        char* base = smem + buf * STG;
#pragma unroll
        for (int i = 0; i < 4; ++i) glds16(asrc[i] + kt * 64, base + (wid + 8 * i) * 1024 + lane * 16);
#pragma unroll
        for (int i = 0; i < 2; ++i) glds16(bsrc[i] + (size_t)kt * bstep, base + 32768 + (wid + 8 * i) * 1024 + lane * 16);
    };

    const unsigned lds0 = (unsigned)(size_t)smem;
    unsigned aaddr[2][4];
#pragma unroll
    for (int m = 0; m < 2; ++m) {
        int row = 64 * wm + 32 * ((i32 >> 2) & 1) + 16 * m + (i32 & 3) + 4 * (i32 >> 3);
        int sw = (row >> 1) & 7;
#pragma unroll
        for (int ks = 0; ks < 4; ++ks) aaddr[m][ks] = lds0 + row * 128 + (((2 * ks + h) ^ sw) << 4);
    }
    unsigned baddr[2][4];
#pragma unroll
    for (int nn = 0; nn < 2; ++nn) {
        if (SSM) {
            int row = 64 * wn + 32 * nn + i32;
            int sw = (row >> 1) & 7;
#pragma unroll
            for (int ks = 0; ks < 4; ++ks) baddr[nn][ks] = lds0 + 32768 + row * 128 + (((2 * ks + h) ^ sw) << 4);
        } else {
            const int blk = (lane >> 4) & 1, q = (lane & 15) >> 2, p = lane & 3;
            const int c = 2 * wn + nn;
#pragma unroll
            for (int t = 0; t < 2; ++t) {
                int row = 8 * h + 4 * t + q;
                int ch = 4 * c + 2 * blk + (p >> 1);
                baddr[nn][t] = lds0 + 32768 + 256 * row + 16 * (ch ^ (((row & 3) << 2) | ((row >> 2) & 3))) + 8 * (p & 1);
            }
            baddr[nn][2] = baddr[nn][3] = 0;
        }
    }

    f32x16 acc[2][2];
#pragma unroll
    for (int m = 0; m < 2; ++m)
#pragma unroll
        for (int nn = 0; nn < 2; ++nn)
#pragma unroll
            for (int r = 0; r < 16; ++r) acc[m][nn][r] = 0.f;

    bf16x8 fa[2][2], fb[2][2];
#define RD_FRAG(s, ks)                                                                                        \
    do {                                                                                                      \
        _Pragma("unroll") for (int m = 0; m < 2; ++m)                                                         \
            asm volatile("ds_read_b128 %0, %1" : "=v"(fa[s][m]) : "v"(aaddr[m][ks] + bo));                    \
        if (SSM) {                                                                                            \
            _Pragma("unroll") for (int nn = 0; nn < 2; ++nn)                                                  \
                asm volatile("ds_read_b128 %0, %1" : "=v"(fb[s][nn]) : "v"(baddr[nn][ks] + bo));              \
        } else {                                                                                              \
            _Pragma("unroll") for (int nn = 0; nn < 2; ++nn) {                                                \
                s16x4 lo_, hi_;                                                                               \
                asm volatile("ds_read_b64_tr_b16 %0, %1 offset:%2" : "=v"(lo_) : "v"(baddr[nn][0] + bo), "i"((ks) * 4096)); \
                asm volatile("ds_read_b64_tr_b16 %0, %1 offset:%2" : "=v"(hi_) : "v"(baddr[nn][1] + bo), "i"((ks) * 4096)); \
                fb[s][nn] = __builtin_shufflevector(lo_, hi_, 0, 1, 2, 3, 4, 5, 6, 7);                        \
            }                                                                                                 \
        }                                                                                                     \
    } while (0)
#define WAIT_FRAG(s, N)                                                                                       \
    asm volatile("s_waitcnt lgkmcnt(" #N ")" : "+v"(fa[s][0]), "+v"(fa[s][1]), "+v"(fb[s][0]), "+v"(fb[s][1]))
#define MM_FRAG(s)                                                                                            \
    do {                                                                                                      \
        _Pragma("unroll") for (int m = 0; m < 2; ++m)                                                         \
            _Pragma("unroll") for (int nn = 0; nn < 2; ++nn) {                                                \
                if (SSM) acc[m][nn] = __builtin_amdgcn_mfma_f32_32x32x16_bf16(fa[s][m], fb[s][nn], acc[m][nn], 0, 0, 0); \
                else     acc[m][nn] = __builtin_amdgcn_mfma_f32_32x32x16_bf16(fb[s][nn], fa[s][m], acc[m][nn], 0, 0, 0); \
            }                                                                                                 \
    } while (0)

    stage(0, 0);
    stage(1, 1);
    float lr = 0.f, li = 0.f, cr = 0.f, ci = 0.f;
    if (SSM) ssm_params(a.lre, a.lim, a.logdt, cb * 64 + wn * 32 + i32, lr, li, cr, ci);
#define K1_STEP(WAITN, DOSTAGE)                                                                            \
    do {                                                                                                      \
        asm volatile("s_waitcnt vmcnt(" #WAITN ")" ::: "memory");                                             \
        __builtin_amdgcn_s_barrier();                                                                         \
        RD_FRAG(0, 0);                                                                                        \
        if (DOSTAGE) stage(kt + 2, nbuf);                                                                     \
        SB;                                                                                                   \
        RD_FRAG(1, 1);                                                                                        \
        if (SSM) { WAIT_FRAG(0, 4); } else { WAIT_FRAG(0, 6); }                                               \
        SB; MM_FRAG(0); SB;                                                                                   \
        RD_FRAG(0, 2);                                                                                        \
        if (SSM) { WAIT_FRAG(1, 4); } else { WAIT_FRAG(1, 6); }                                               \
        SB; MM_FRAG(1); SB;                                                                                   \
        RD_FRAG(1, 3);                                                                                        \
        if (SSM) { WAIT_FRAG(0, 4); } else { WAIT_FRAG(0, 6); }                                               \
        SB; MM_FRAG(0); SB;                                                                                   \
        WAIT_FRAG(1, 0);                                                                                      \
        SB; MM_FRAG(1); SB;                                                                                   \
    } while (0)
    int bo = 0, nbuf = 2;
    int kt = 0;
#pragma unroll 1
    for (; kt < 6; ++kt) {
        K1_STEP(6, true);
        bo = (bo == 2 * STG) ? 0 : bo + STG;
        nbuf = (nbuf == 2) ? 0 : nbuf + 1;
    }
    K1_STEP(6, false);
    bo = (bo == 2 * STG) ? 0 : bo + STG;
    K1_STEP(0, false);

    __builtin_amdgcn_s_barrier();
    if (SSM) {
        const int chan = cb * 64 + wn * 32 + i32;
        float hr = 0.f, hi = 0.f;
        const int tb = 64 * wm + 32 * h;
        const int odd = i32 & 1;
        const int cw = 64 * wn + 32 * odd + (i32 & ~1);
#pragma unroll
        for (int m = 0; m < 2; ++m)
#pragma unroll
            for (int r = 0; r < 16; ++r) {
                float ur = acc[m][0][r], ui = acc[m][1][r];
                float br = cr * ur - ci * ui, bi = cr * ui + ci * ur;
                float nr = lr * hr - li * hi + br;
                float ni = lr * hi + li * hr + bi;
                hr = nr; hi = ni;
                const float snd = odd ? hr : -hi;
                const float rcv = __builtin_bit_cast(float, __builtin_amdgcn_mov_dpp(__builtin_bit_cast(int, snd), 0xB1, 0xF, 0xF, true));
                const unsigned pk = odd ? pack2(rcv, -hi) : pack2(hr, rcv);
                const int t = tb + 16 * m + r;
                *(unsigned*)(smem + t * 256 + (((cw >> 3) ^ ((t >> 2) & 15)) << 4) + (cw & 7) * 2) = pk;
            }
        float e0r = __shfl(hr, i32), e0i = __shfl(hi, i32);
        const int chunk = (row0 >> 6) + wm;
        float pr = lr, pi = li;
#pragma unroll
        for (int s = 0; s < 5; ++s) csq(pr, pi);
        if (h == 0) {
            a.e32[chunk * P_ + chan] = make_float2(hr, hi);
        } else {
            *(f32x2*)(smem + 65536 + (wm * 64 + wn * 32 + i32) * 8) = (f32x2){hr + pr * e0r - pi * e0i, hi + pr * e0i + pi * e0r};
            csq(pr, pi);
            if (wm == 0) *(f32x2*)(smem + 65536 + 2048 + (wn * 32 + i32) * 8) = (f32x2){pr, pi};
        }
    } else {
        const int n0 = (cb - 8) * 128;
        u32x2 bb[2][4];
#pragma unroll
        for (int nn = 0; nn < 2; ++nn)
#pragma unroll
            for (int q = 0; q < 4; ++q) bb[nn][q] = *(const u32x2*)(a.b1 + n0 + 64 * wn + 32 * nn + 8 * q + 4 * h);
#pragma unroll
        for (int nn = 0; nn < 2; ++nn) {
#pragma unroll
            for (int q = 0; q < 4; ++q) {
                const int nc = 8 * wn + 4 * nn + q;
                float bv[4] = {bits2f(bb[nn][q][0] << 16), bits2f(bb[nn][q][0] & 0xffff0000u),
                               bits2f(bb[nn][q][1] << 16), bits2f(bb[nn][q][1] & 0xffff0000u)};
#pragma unroll
                for (int m = 0; m < 2; ++m) {
                    const int t = 64 * wm + 32 * ((i32 >> 2) & 1) + 16 * m + (i32 & 3) + 4 * (i32 >> 3);
                    f32x2 g0 = gelu2((f32x2){acc[m][nn][4 * q], acc[m][nn][4 * q + 1]} + (f32x2){bv[0], bv[1]});
                    f32x2 g1 = gelu2((f32x2){acc[m][nn][4 * q + 2], acc[m][nn][4 * q + 3]} + (f32x2){bv[2], bv[3]});
                    u32x2 o; o[0] = pack2(g0[0], g0[1]); o[1] = pack2(g1[0], g1[1]);
                    *(u32x2*)(smem + t * 256 + ((nc ^ ((t >> 2) & 15)) << 4) + 8 * h) = o;
                }
            }
        }
    }
    __syncthreads();
    if (SSM && tid < 64) {
        const f32x2 q = *(const f32x2*)(smem + 65536 + 2048 + tid * 8);
        const int chan = cb * 64 + tid;
        float cr_ = 0.f, ci_ = 0.f;
#pragma unroll
        for (int w = 0; w < 4; ++w) {
            a.e64[(rb * 4 + w) * P_ + chan] = make_float2(cr_, ci_);
            const f32x2 e = *(const f32x2*)(smem + 65536 + (w * 64 + tid) * 8);
            const float nr = q[0] * cr_ - q[1] * ci_ + e[0];
            const float ni = q[0] * ci_ + q[1] * cr_ + e[1];
            cr_ = nr; ci_ = ni;
        }
        a.e256[rb * P_ + chan] = make_float2(cr_, ci_);
    }
    {
        const int i16 = lane & 15, g4 = lane >> 4;
#pragma unroll
        for (int it = 0; it < 8; ++it) {
            const int f = it * 8 + wid;
            const int rtl = f >> 2, ksl = f & 3;
            const int t = 64 * (rtl >> 2) + 4 * i16 + (rtl & 3);
            u32x4 v = *(const u32x4*)(smem + t * 256 + (((4 * ksl + g4) ^ ((t >> 2) & 15)) << 4));
            const int rt = rb * 16 + rtl;
            if (SSM) {
                const int ks = (ksl & 1) * 16 + 2 * cb + (ksl >> 1);
                *(u32x4*)((char*)a.ha + ((size_t)(rt * 32 + ks) * 64 + lane) * 16) = v;
            } else {
                const int ks = (cb - 8) * 4 + ksl;
                *(u32x4*)((char*)a.hm + ((size_t)(rt * 56 + ks) * 64 + lane) * 16) = v;
            }
        }
    }
}

__global__ __launch_bounds__(512, 2) void k1_kernel(K1Args a) {
    __shared__ __attribute__((aligned(16))) char smem[3 * STG];
    const int bid = blockIdx.x;
    const int tid = threadIdx.x, wid = tid >> 6, lane = tid & 63;
    if (bid < 192) {
        const int xcd = bid & 7, idx = bid >> 3;
        const int cg = xcd >> 1, ci = idx >> 2;
        const int rb = (xcd & 1) * 4 + (idx & 3);
        const int cb = (cg < 2 ? cg * 6 : 2 + cg * 5) + ci;
        if (cg >= 2 && ci == 5) return;
        if (cb < 8) k1_gemm<true>(a, smem, rb, cb);
        else        k1_gemm<false>(a, smem, rb, cb);
        return;
    }
    const int sid = bid - 192;
    if (sid < 28) {
        const int k0 = sid * 64;
        unsigned short* sm = (unsigned short*)smem;
#pragma unroll
        for (int it = 0; it < 8; ++it) {
            int idx = it * 512 + tid;
            int r = idx >> 6, c = idx & 63;
            u32x4 v = *(const u32x4*)(a.w2 + (size_t)(k0 + r) * D_ + c * 8);
            *(u32x4*)(sm + r * 520 + c * 8) = v;
        }
        __syncthreads();
#pragma unroll
        for (int it = 0; it < 8; ++it) {
            const int f = it * 8 + wid;
            const int nt = f >> 1, ksl = f & 1;
            const int n = 16 * nt + (lane & 15), kb = 32 * ksl + 8 * (lane >> 4);
            unsigned short v[8];
#pragma unroll
            for (int e = 0; e < 8; ++e) v[e] = sm[(kb + e) * 520 + n];
            u32x4 o;
#pragma unroll
            for (int e = 0; e < 4; ++e) o[e] = (unsigned)v[2 * e] | ((unsigned)v[2 * e + 1] << 16);
            *(u32x4*)((char*)a.w2t + ((size_t)(nt * 56 + (k0 >> 5) + ksl) * 64 + lane) * 16) = o;
        }
        return;
    }
    if (sid >= 61) {
#pragma unroll
        for (int it = 0; it < 16; ++it) {
            const int f = (sid - 61) * 128 + it * 8 + wid;
            const int nt = f >> 5, ks = f & 31;
            const bf16* src = (ks < 16 ? a.Cre : a.Cim) + (size_t)(16 * nt + (lane & 15)) * P_ + 32 * (ks & 15) + 8 * (lane >> 4);
            *(u32x4*)((char*)a.cf + ((size_t)f * 64 + lane) * 16) = *(const u32x4*)src;
        }
        return;
    }
    if (sid < 60) {
        const int r0 = (sid - 28) * 64 + wid * 8;
        u32x4 gv = *(const u32x4*)(a.gw + lane * 8);
        u32x4 xv[8];
#pragma unroll
        for (int r = 0; r < 8; ++r) xv[r] = *(const u32x4*)(a.x + (size_t)(r0 + r) * D_ + lane * 8);
        float gwf[8];
#pragma unroll
        for (int e = 0; e < 4; ++e) { gwf[2 * e] = bits2f(gv[e] << 16); gwf[2 * e + 1] = bits2f(gv[e] & 0xffff0000u); }
        float mine = 0.f;
#pragma unroll
        for (int r = 0; r < 8; ++r) {
            float s = 0.f;
#pragma unroll
            for (int e = 0; e < 4; ++e) {
                s += rbf(bits2f(xv[r][e] << 16) * gwf[2 * e]);
                s += rbf(bits2f(xv[r][e] & 0xffff0000u) * gwf[2 * e + 1]);
            }
#pragma unroll
            for (int o = 32; o > 0; o >>= 1) s += __shfl_xor(s, o);
            if (lane == r) mine = s;
        }
        if (lane < 8) {
            float gs = rbf(mine);
            a.gate[r0 + lane] = rbf(1.0f / (1.0f + expf(-gs)));
        }
        return;
    }
    {
        const int p = tid;
        float lr, li, t0, t1;
        ssm_params(a.lre, a.lim, a.logdt, p, lr, li, t0, t1);
        a.lam[p] = make_float2(lr, li);
        csq(lr, li); csq(lr, li);
        a.lam[512 + p] = make_float2(lr, li);
        csq(lr, li); csq(lr, li); csq(lr, li);
        a.lam[1024 + p] = make_float2(lr, li);
        csq(lr, li);
        a.lam[1536 + p] = make_float2(lr, li);
        csq(lr, li); csq(lr, li);
        a.lam[2048 + p] = make_float2(lr, li);
        a.e256[p - P_] = make_float2(0.f, 0.f);
    }
}

struct K2Args {
    const bf16* x; const bf16* cf;
    const bf16* Dsk; const bf16* b2;
    const bf16* ha; const bf16* hm; const bf16* w2t; const float2* e64; const float2* e32; const float* gate;
    const float2* lam; const float2* e256;
    float* out;
};

#define ZS_STRIDE 4112
#define LAM_OFF (16 * ZS_STRIDE)

#define K2_SETUP_LOADS()                                                                             \
    f32x2 ev[7];                                                                                     \
    {                                                                                                \
        const f32x2* ep = (const f32x2*)a.e256 + tid;                                                \
        _Pragma("unroll") for (int i = 0; i < 7; ++i) {                                              \
            int j = (rb >> 2) - 1 - i; j = j < -1 ? -1 : j;                                          \
            ev[i] = ep[j * P_];                                                                      \
        }                                                                                            \
    }                                                                                                \
    const f32x2 pfx = *((const f32x2*)a.e64 + (size_t)rb * P_ + tid);                                \
    const f32x2 e32v = *((const f32x2*)a.e32 + (size_t)rb * P_ + tid);                               \
    const f32x2 l1 = *((const f32x2*)a.lam + tid);                                                   \
    const f32x2 l4 = *((const f32x2*)a.lam + 512 + tid);                                             \
    const f32x2 l32 = *((const f32x2*)a.lam + 1024 + tid);                                           \
    const f32x2 l64 = *((const f32x2*)a.lam + 1536 + tid);                                           \
    const f32x2 l256 = *((const f32x2*)a.lam + 2048 + tid);
#define K2_SETUP_COMPUTE()                                                                           \
    {                                                                                                \
        float Cr = 0.f, Ci = 0.f;                                                                    \
        _Pragma("unroll") for (int i = 6; i >= 0; --i) {                                             \
            float nr = l256[0] * Cr - l256[1] * Ci + ev[i][0];                                       \
            float ni = l256[0] * Ci + l256[1] * Cr + ev[i][1];                                       \
            Cr = nr; Ci = ni;                                                                        \
        }                                                                                            \
        _Pragma("unroll") for (int i = 0; i < 3; ++i) {                                              \
            float nr = l64[0] * Cr - l64[1] * Ci;                                                    \
            float ni = l64[0] * Ci + l64[1] * Cr;                                                    \
            const bool u = i < (rb & 3);                                                             \
            Cr = u ? nr : Cr; Ci = u ? ni : Ci;                                                      \
        }                                                                                            \
        Cr += pfx[0]; Ci += pfx[1];                                                                  \
        const float c1r = e32v[0] + l32[0] * Cr - l32[1] * Ci;                                       \
        const float c1i = e32v[1] + l32[0] * Ci + l32[1] * Cr;                                       \
        _Pragma("unroll") for (int sub = 0; sub < 2; ++sub) {                                        \
            const float cr_ = sub ? c1r : Cr, ci_ = sub ? c1i : Ci;                                  \
            float zr = cr_ * l1[0] - ci_ * l1[1];                                                    \
            float zi = cr_ * l1[1] + ci_ * l1[0];                                                    \
            _Pragma("unroll") for (int ii = 0; ii < 8; ++ii) {                                       \
                *(f32x2*)(smem + (sub * 8 + ii) * ZS_STRIDE + tid * 8) = (f32x2){zr, zi};            \
                float nr = zr * l4[0] - zi * l4[1];                                                  \
                float ni = zr * l4[1] + zi * l4[0];                                                  \
                zr = nr; zi = ni;                                                                    \
            }                                                                                        \
        }                                                                                            \
        *(f32x2*)(smem + LAM_OFF + tid * 8) = l1;                                                    \
    }                                                                                                \
    asm volatile("s_waitcnt lgkmcnt(0)" ::: "memory");                                               \
    __builtin_amdgcn_s_barrier();

__global__ __launch_bounds__(512, 2) void k2_kernel(K2Args a) {
    __shared__ __attribute__((aligned(16))) char smem[131072];
    const int bid = blockIdx.x;
    const int tid = threadIdx.x, wid = tid >> 6, lane = tid & 63;
    const int idx = bid >> 3;
    const int rb = (bid & 7) * 4 + (idx >> 3), cbk = idx & 7;
    const int row0 = rb * 64, col0 = cbk * 64;
    const int i16 = lane & 15, g4 = lane >> 4;
    const int widu = __builtin_amdgcn_readfirstlane(wid);
    const int voff = lane * 16;

    f32x4 acc[4][4];
#pragma unroll
    for (int m = 0; m < 4; ++m)
#pragma unroll
        for (int n = 0; n < 4; ++n) acc[m][n] = (f32x4){0.f, 0.f, 0.f, 0.f};

    if (widu < 4) {
        const __amdgpu_buffer_rsrc_t ra = __builtin_amdgcn_make_buffer_rsrc((void*)a.hm, 0, 128 * 56 * 1024, 0x00020000);
        const __amdgpu_buffer_rsrc_t rbs = __builtin_amdgcn_make_buffer_rsrc((void*)a.w2t, 0, 32 * 56 * 1024, 0x00020000);
        const int abase = (rb * 4 * 56 + widu * 14) * 1024, bbase = (cbk * 4 * 56 + widu * 14) * 1024;
        bf16x8 A[4][4], B[4][4];
#define K2LD(bi, s)                                                                                  \
        do {                                                                                         \
            _Pragma("unroll") for (int m = 0; m < 4; ++m)                                            \
                A[bi][m] = __builtin_bit_cast(bf16x8, __builtin_amdgcn_raw_buffer_load_b128(ra, voff, abase + (m * 56 + (s)) * 1024, 0)); \
            _Pragma("unroll") for (int n = 0; n < 4; ++n)                                            \
                B[bi][n] = __builtin_bit_cast(bf16x8, __builtin_amdgcn_raw_buffer_load_b128(rbs, voff, bbase + (n * 56 + (s)) * 1024, 0)); \
        } while (0)
#define K2MM(bi)                                                                                     \
        do {                                                                                         \
            _Pragma("unroll") for (int m = 0; m < 4; ++m)                                            \
                _Pragma("unroll") for (int n = 0; n < 4; ++n)                                        \
                    acc[m][n] = __builtin_amdgcn_mfma_f32_16x16x32_bf16(A[bi][m], B[bi][n], acc[m][n], 0, 0, 0); \
        } while (0)
        K2_SETUP_LOADS();
        SB;
        K2LD(0, 0); K2LD(1, 1); K2LD(2, 2);
        SB;
        K2_SETUP_COMPUTE();
        SB;
#pragma unroll
        for (int s = 0; s < 14; ++s) {
            SB;
            if (s + 3 < 14) K2LD((s + 3) % 4, s + 3);
            SB;
            K2MM(s % 4);
        }
        SB;
    } else {
        const int wz = widu - 4;
        const __amdgpu_buffer_rsrc_t ra = __builtin_amdgcn_make_buffer_rsrc((void*)a.ha, 0, 128 * 32 * 1024, 0x00020000);
        const __amdgpu_buffer_rsrc_t rbs = __builtin_amdgcn_make_buffer_rsrc((void*)a.cf, 0, 32 * 32 * 1024, 0x00020000);
        const int abase = (rb * 4 * 32 + wz * 4) * 1024, bbase = (cbk * 4 * 32 + wz * 4) * 1024;
        bf16x8 Hr[4], Hi[4], Br[2][4], Bi[2][4];
#define HLD(sl)                                                                                      \
        do {                                                                                         \
            _Pragma("unroll") for (int m = 0; m < 4; ++m) {                                          \
                Hr[m] = __builtin_bit_cast(bf16x8, __builtin_amdgcn_raw_buffer_load_b128(ra, voff, abase + (m * 32 + (sl)) * 1024, 0)); \
                Hi[m] = __builtin_bit_cast(bf16x8, __builtin_amdgcn_raw_buffer_load_b128(ra, voff, abase + (m * 32 + 16 + (sl)) * 1024, 0)); \
            }                                                                                        \
        } while (0)
#define ZLD(bi, sl)                                                                                  \
        do {                                                                                         \
            _Pragma("unroll") for (int n = 0; n < 4; ++n) {                                          \
                Br[bi][n] = __builtin_bit_cast(bf16x8, __builtin_amdgcn_raw_buffer_load_b128(rbs, voff, bbase + (n * 32 + (sl)) * 1024, 0)); \
                Bi[bi][n] = __builtin_bit_cast(bf16x8, __builtin_amdgcn_raw_buffer_load_b128(rbs, voff, bbase + (n * 32 + 16 + (sl)) * 1024, 0)); \
            }                                                                                        \
        } while (0)
        K2_SETUP_LOADS();
        SB;
        HLD(0); ZLD(0, 0); ZLD(1, 1);
        SB;
        K2_SETUP_COMPUTE();
        SB;
        const char* zt = smem + i16 * ZS_STRIDE + (128 * wz + 8 * g4) * 8;
        const char* lt = smem + LAM_OFF + (128 * wz + 8 * g4) * 8;
#pragma unroll
        for (int sl = 0; sl < 4; ++sl) {
            const int cur = sl & 1;
            SB;
#pragma unroll
            for (int m = 0; m < 4; ++m)
#pragma unroll
                for (int n = 0; n < 4; ++n) {
                    acc[m][n] = __builtin_amdgcn_mfma_f32_16x16x32_bf16(Hr[m], Br[cur][n], acc[m][n], 0, 0, 0);
                    acc[m][n] = __builtin_amdgcn_mfma_f32_16x16x32_bf16(Hi[m], Bi[cur][n], acc[m][n], 0, 0, 0);
                }
            SB;
            if (sl + 1 < 4) HLD(sl + 1);
            SB;
            f32x4 z[4], lm[4];
#pragma unroll
            for (int q = 0; q < 4; ++q) {
                z[q] = *(const f32x4*)(zt + sl * 256 + q * 16);
                lm[q] = *(const f32x4*)(lt + sl * 256 + q * 16);
            }
#pragma unroll
            for (int m = 0; m < 4; ++m) {
                u32x4 zr, zi;
#pragma unroll
                for (int q = 0; q < 4; ++q) {
                    zr[q] = pack2(z[q][0], z[q][2]);
                    zi[q] = pack2(-z[q][1], -z[q][3]);
                    if (m < 3) {
                        float nr0 = z[q][0] * lm[q][0] - z[q][1] * lm[q][1];
                        float ni0 = z[q][0] * lm[q][1] + z[q][1] * lm[q][0];
                        float nr1 = z[q][2] * lm[q][2] - z[q][3] * lm[q][3];
                        float ni1 = z[q][2] * lm[q][3] + z[q][3] * lm[q][2];
                        z[q] = (f32x4){nr0, ni0, nr1, ni1};
                    }
                }
                bf16x8 Zr = __builtin_bit_cast(bf16x8, zr), Zi = __builtin_bit_cast(bf16x8, zi);
#pragma unroll
                for (int n = 0; n < 4; ++n) {
                    acc[m][n] = __builtin_amdgcn_mfma_f32_16x16x32_bf16(Zr, Br[cur][n], acc[m][n], 0, 0, 0);
                    acc[m][n] = __builtin_amdgcn_mfma_f32_16x16x32_bf16(Zi, Bi[cur][n], acc[m][n], 0, 0, 0);
                }
            }
            SB;
            if (sl + 2 < 4) ZLD(cur, sl + 2);
        }
        SB;
    }
    float gv[2][4], xv[2][4], dsk[2], bb[2];
#pragma unroll
    for (int f = 0; f < 2; ++f) {
        const int fr = wid * 2 + f;
        const int m = fr >> 2, n = fr & 3;
        const int d = col0 + 16 * n + i16;
        dsk[f] = b2f(a.Dsk[d]); bb[f] = b2f(a.b2[d]);
#pragma unroll
        for (int r = 0; r < 4; ++r) {
            const int t = row0 + 16 * g4 + 4 * r + m;
            gv[f][r] = a.gate[t];
            xv[f][r] = b2f(a.x[(size_t)t * D_ + d]);
        }
    }
    __syncthreads();
#pragma unroll
    for (int m = 0; m < 4; ++m)
#pragma unroll
        for (int n = 0; n < 4; ++n)
            *(f32x4*)(smem + ((wid * 16 + m * 4 + n) * 64 + lane) * 16) = acc[m][n];
    __syncthreads();
#pragma unroll
    for (int f = 0; f < 2; ++f) {
        const int fr = wid * 2 + f;
        const int m = fr >> 2, n = fr & 3;
        f32x4 sm_ = (f32x4){0.f, 0.f, 0.f, 0.f}, ss = (f32x4){0.f, 0.f, 0.f, 0.f};
#pragma unroll
        for (int w = 0; w < 4; ++w) {
            sm_ += *(const f32x4*)(smem + ((w * 16 + fr) * 64 + lane) * 16);
            ss += *(const f32x4*)(smem + (((w + 4) * 16 + fr) * 64 + lane) * 16);
        }
        const int d = col0 + 16 * n + i16;
#pragma unroll
        for (int r = 0; r < 4; ++r) {
            const int t = row0 + 16 * g4 + 4 * r + m;
            const float g = gv[f][r];
            const float av = 2.0f * ss[r] + rbf(xv[f][r] * dsk[f]);
            const float bv = rbf(rbf(sm_[r]) + bb[f]);
            a.out[(size_t)t * D_ + d] = g * av + rbf(rbf(1.0f - g) * bv);
        }
    }
}

extern "C" void kernel_launch(void* const* d_in, const int* in_sizes, int n_in,
                              void* d_out, int out_size, void* d_ws, size_t ws_size,
                              hipStream_t stream) {
    char* ws = (char*)d_ws;
    K1Args a1;
    a1.x = (const bf16*)d_in[0]; a1.lre = (const bf16*)d_in[1]; a1.lim = (const bf16*)d_in[2];
    a1.Bre = (const bf16*)d_in[3]; a1.Bim = (const bf16*)d_in[4]; a1.logdt = (const bf16*)d_in[7];
    a1.w1 = (const bf16*)d_in[10]; a1.b1 = (const bf16*)d_in[11]; a1.w2 = (const bf16*)d_in[12];
    a1.gw = (const bf16*)d_in[14];
    a1.Cre = (const bf16*)d_in[5]; a1.Cim = (const bf16*)d_in[6]; a1.cf = (bf16*)(ws + WS_CF);
    a1.ha = (bf16*)(ws + WS_HA); a1.hm = (bf16*)(ws + WS_HM); a1.w2t = (bf16*)(ws + WS_W2T);
    a1.e64 = (float2*)(ws + WS_E64); a1.e32 = (float2*)(ws + WS_E32); a1.gate = (float*)(ws + WS_G);
    a1.lam = (float2*)(ws + WS_LAM); a1.e256 = (float2*)(ws + WS_E256);
    K2Args a2;
    a2.x = a1.x;
    a2.cf = a1.cf; a2.Dsk = (const bf16*)d_in[8];
    a2.b2 = (const bf16*)d_in[13];
    a2.ha = a1.ha; a2.hm = a1.hm; a2.w2t = a1.w2t; a2.e64 = a1.e64; a2.e32 = a1.e32; a2.gate = a1.gate;
    a2.lam = a1.lam; a2.e256 = a1.e256;
    a2.out = (float*)d_out;
    k1_kernel<<<261, 512, 0, stream>>>(a1);
    k2_kernel<<<256, 512, 0, stream>>>(a2);
}
```

```cpp
#include <hip/hip_runtime.h>
#include <hip/hip_bf16.h>
#include <stdint.h>

typedef __hip_bfloat16 bf16;
typedef __attribute__((ext_vector_type(8))) short bf16x8;
typedef __attribute__((ext_vector_type(4))) short s16x4;
typedef __attribute__((ext_vector_type(4))) float f32x4;
typedef __attribute__((ext_vector_type(2))) float f32x2;
typedef __attribute__((ext_vector_type(16))) float f32x16;
typedef __attribute__((ext_vector_type(4))) unsigned u32x4;
typedef __attribute__((ext_vector_type(2))) unsigned u32x2;

#define L_ 2048
#define D_ 512
#define P_ 512
#define F_ 1792

#define WS_HA   0ull
#define WS_CF   (4ull << 20)
#define WS_HM   (8ull << 20)
#define WS_W2T  (16ull << 20)
#define WS_E64  ((18ull << 20) + 4096)
#define WS_E32  ((18ull << 20) + 139264)
#define WS_G    ((18ull << 20) + 270336)
#define WS_LAM  ((18ull << 20) + 280576)
#define WS_E256 ((18ull << 20) + 307200)

#define SB __builtin_amdgcn_sched_barrier(0)

__device__ __forceinline__ float bits2f(unsigned u) { return __uint_as_float(u); }
__device__ __forceinline__ float b2f(bf16 v) { return __bfloat162float(v); }
typedef __attribute__((ext_vector_type(2))) __bf16 nbf16x2;
__device__ __forceinline__ unsigned pack2(float lo, float hi) {
    nbf16x2 v = {(__bf16)lo, (__bf16)hi};
    return __builtin_bit_cast(unsigned, v);
}
__device__ __forceinline__ float rbf(float v) { return bits2f(pack2(v, v) << 16); }

__device__ __forceinline__ void glds16(const void* src, void* lds) {
    __builtin_amdgcn_global_load_lds((const unsigned*)src, (unsigned*)lds, 16, 0, 0);
}

__device__ __forceinline__ void ssm_params(const bf16* lre, const bf16* lim, const bf16* logdt, int p,
                                           float& br, float& bi, float& cr, float& ci) {
    float lr = b2f(lre[p]), li = b2f(lim[p]);
    float dt = rbf(expf(b2f(logdt[p])));
    float ar = dt * lr, ai = dt * li;
    float e = expf(ar);
    float s, c;
    sincosf(ai, &s, &c);
    br = e * c; bi = e * s;
    float nr = br - 1.0f, ni = bi;
    float den = lr * lr + li * li;
    cr = (nr * lr + ni * li) / den;
    ci = (ni * lr - nr * li) / den;
}
__device__ __forceinline__ void csq(float& r, float& i) { float a = r * r - i * i, b = 2.0f * r * i; r = a; i = b; }

__device__ __forceinline__ float gelu_tanh(float v) {
    const float c0 = -2.0f * 1.4426950408889634f * 0.7978845608028654f;
    const float c1 = c0 * 0.044715f;
    float t = __builtin_fmaf(c1, v * v, c0);
    float e = __builtin_amdgcn_exp2f(v * t);
    return v * __builtin_amdgcn_rcpf(1.0f + e);
}

__device__ __forceinline__ f32x2 gelu2(f32x2 v) {
    const float c0 = -2.0f * 1.4426950408889634f * 0.7978845608028654f;
    const float c1 = c0 * 0.044715f;
    f32x2 t = (v * v) * c1 + c0;
    f32x2 a2 = v * t;
    f32x2 d = (f32x2){__builtin_amdgcn_exp2f(a2[0]), __builtin_amdgcn_exp2f(a2[1])} + 1.0f;
    return v * (f32x2){__builtin_amdgcn_rcpf(d[0]), __builtin_amdgcn_rcpf(d[1])};
}

#define STG 49152
#define BOFS(buf) ((buf) * STG)

struct K1Args {
    const bf16* x; const bf16* lre; const bf16* lim; const bf16* Bre; const bf16* Bim; const bf16* logdt;
    const bf16* w1; const bf16* b1; const bf16* w2; const bf16* gw; const bf16* Cre; const bf16* Cim;
    bf16* cf; bf16* ha; bf16* hm; bf16* w2t; float2* e64; float2* e32; float* gate; float2* lam; float2* e256;
};

template <bool SSM>
__device__ __forceinline__ void k1_gemm(const K1Args& a, char* smem, int rb, int cb) {
    const int tid = threadIdx.x, wid = tid >> 6, lane = tid & 63;
    constexpr int NW = SSM ? 4 : 8;
    constexpr int BM = SSM ? 128 : 256;
    constexpr int NB = SSM ? 4 : 2;
    constexpr int BOFF = BM * 128;
    const int wm = SSM ? (wid & 1) : (wid & 3), wn = SSM ? (wid >> 1) : (wid >> 2);
    const int row0 = rb * BM;
    const int h = lane >> 5, i32 = lane & 31;

    const bf16* asrc[4];
    const bf16* bsrc[NB];
#pragma unroll
    for (int i = 0; i < 4; ++i) {
        int j = wid + NW * i;
        int row = 8 * j + (lane >> 3);
        int ch = (lane & 7) ^ ((row >> 1) & 7);
        asrc[i] = a.x + (size_t)(row0 + row) * D_ + ch * 8;
    }
#pragma unroll
    for (int i = 0; i < NB; ++i) {
        int j = wid + NW * i;
        if (SSM) {
            int n = 8 * j + (lane >> 3);
            int ch = (lane & 7) ^ ((n >> 1) & 7);
            int chan = cb * 64 + (n >> 6) * 32 + (n & 31);
            const bf16* base = ((n >> 5) & 1) ? a.Bim : a.Bre;
            bsrc[i] = base + (size_t)chan * D_ + ch * 8;
        } else {
            int k = 4 * j + (lane >> 4);
            int ch = (lane & 15) ^ (((k & 3) << 2) | ((k >> 2) & 3));
            bsrc[i] = a.w1 + (size_t)k * F_ + (cb - 8) * 128 + ch * 8;
        }
    }
    const int bstep = SSM ? 64 : 64 * F_;

    auto stage = [&](int kt, int buf) {
        char* base = smem + buf * STG;
#pragma unroll
        for (int i = 0; i < 4; ++i) glds16(asrc[i] + kt * 64, base + (wid + NW * i) * 1024 + lane * 16);
#pragma unroll
        for (int i = 0; i < NB; ++i) glds16(bsrc[i] + (size_t)kt * bstep, base + BOFF + (wid + NW * i) * 1024 + lane * 16);
    };

    const unsigned lds0 = (unsigned)(size_t)smem;
    unsigned aaddr[2][4];
#pragma unroll
    for (int m = 0; m < 2; ++m) {
        int row = 64 * wm + 32 * ((i32 >> 2) & 1) + 16 * m + (i32 & 3) + 4 * (i32 >> 3);
        int sw = (row >> 1) & 7;
#pragma unroll
        for (int ks = 0; ks < 4; ++ks) aaddr[m][ks] = lds0 + row * 128 + (((2 * ks + h) ^ sw) << 4);
    }
    unsigned baddr[2][4];
#pragma unroll
    for (int nn = 0; nn < 2; ++nn) {
        if (SSM) {
            int row = 64 * wn + 32 * nn + i32;
            int sw = (row >> 1) & 7;
#pragma unroll
            for (int ks = 0; ks < 4; ++ks) baddr[nn][ks] = lds0 + BOFF + row * 128 + (((2 * ks + h) ^ sw) << 4);
        } else {
            const int blk = (lane >> 4) & 1, q = (lane & 15) >> 2, p = lane & 3;
            const int c = 2 * wn + nn;
#pragma unroll
            for (int t = 0; t < 2; ++t) {
                int row = 8 * h + 4 * t + q;
                int ch = 4 * c + 2 * blk + (p >> 1);
                baddr[nn][t] = lds0 + BOFF + 256 * row + 16 * (ch ^ (((row & 3) << 2) | ((row >> 2) & 3))) + 8 * (p & 1);
            }
            baddr[nn][2] = baddr[nn][3] = 0;
        }
    }

    f32x16 acc[2][2];
#pragma unroll
    for (int m = 0; m < 2; ++m)
#pragma unroll
        for (int nn = 0; nn < 2; ++nn)
#pragma unroll
            for (int r = 0; r < 16; ++r) acc[m][nn][r] = 0.f;

    bf16x8 fa[2][2], fb[2][2];
#define RD_FRAG(s, ks)                                                                                        \
    do {                                                                                                      \
        _Pragma("unroll") for (int m = 0; m < 2; ++m)                                                         \
            asm volatile("ds_read_b128 %0, %1" : "=v"(fa[s][m]) : "v"(aaddr[m][ks] + bo));                    \
        if (SSM) {                                                                                            \
            _Pragma("unroll") for (int nn = 0; nn < 2; ++nn)                                                  \
                asm volatile("ds_read_b128 %0, %1" : "=v"(fb[s][nn]) : "v"(baddr[nn][ks] + bo));              \
        } else {                                                                                              \
            _Pragma("unroll") for (int nn = 0; nn < 2; ++nn) {                                                \
                s16x4 lo_, hi_;                                                                               \
                asm volatile("ds_read_b64_tr_b16 %0, %1 offset:%2" : "=v"(lo_) : "v"(baddr[nn][0] + bo), "i"((ks) * 4096)); \
                asm volatile("ds_read_b64_tr_b16 %0, %1 offset:%2" : "=v"(hi_) : "v"(baddr[nn][1] + bo), "i"((ks) * 4096)); \
                fb[s][nn] = __builtin_shufflevector(lo_, hi_, 0, 1, 2, 3, 4, 5, 6, 7);                        \
            }                                                                                                 \
        }                                                                                                     \
    } while (0)
#define WAIT_FRAG(s, N)                                                                                       \
    asm volatile("s_waitcnt lgkmcnt(" #N ")" : "+v"(fa[s][0]), "+v"(fa[s][1]), "+v"(fb[s][0]), "+v"(fb[s][1]))
#define MM_FRAG(s)                                                                                            \
    do {                                                                                                      \
        _Pragma("unroll") for (int m = 0; m < 2; ++m)                                                         \
            _Pragma("unroll") for (int nn = 0; nn < 2; ++nn) {                                                \
                if (SSM) acc[m][nn] = __builtin_amdgcn_mfma_f32_32x32x16_bf16(fa[s][m], fb[s][nn], acc[m][nn], 0, 0, 0); \
                else     acc[m][nn] = __builtin_amdgcn_mfma_f32_32x32x16_bf16(fb[s][nn], fa[s][m], acc[m][nn], 0, 0, 0); \
            }                                                                                                 \
    } while (0)

    stage(0, 0);
    stage(1, 1);
    float lr = 0.f, li = 0.f, cr = 0.f, ci = 0.f;
    if (SSM) ssm_params(a.lre, a.lim, a.logdt, cb * 64 + wn * 32 + i32, lr, li, cr, ci);
#define K1_STEP(WAITN, DOSTAGE)                                                                            \
    do {                                                                                                      \
        if (WAITN == 0) asm volatile("s_waitcnt vmcnt(0)" ::: "memory");                                      \
        else if (SSM) asm volatile("s_waitcnt vmcnt(8)" ::: "memory");                                        \
        else asm volatile("s_waitcnt vmcnt(6)" ::: "memory");                                                 \
        __builtin_amdgcn_s_barrier();                                                                         \
        RD_FRAG(0, 0);                                                                                        \
        if (DOSTAGE) stage(kt + 2, nbuf);                                                                     \
        SB;                                                                                                   \
        RD_FRAG(1, 1);                                                                                        \
        if (SSM) { WAIT_FRAG(0, 4); } else { WAIT_FRAG(0, 6); }                                               \
        SB; MM_FRAG(0); SB;                                                                                   \
        RD_FRAG(0, 2);                                                                                        \
        if (SSM) { WAIT_FRAG(1, 4); } else { WAIT_FRAG(1, 6); }                                               \
        SB; MM_FRAG(1); SB;                                                                                   \
        RD_FRAG(1, 3);                                                                                        \
        if (SSM) { WAIT_FRAG(0, 4); } else { WAIT_FRAG(0, 6); }                                               \
        SB; MM_FRAG(0); SB;                                                                                   \
        WAIT_FRAG(1, 0);                                                                                      \
        SB; MM_FRAG(1); SB;                                                                                   \
    } while (0)
    int bo = 0, nbuf = 2;
    int kt = 0;
#pragma unroll 1
    for (; kt < 6; ++kt) {
        K1_STEP(6, true);
        bo = (bo == 2 * STG) ? 0 : bo + STG;
        nbuf = (nbuf == 2) ? 0 : nbuf + 1;
    }
    K1_STEP(6, false);
    bo = (bo == 2 * STG) ? 0 : bo + STG;
    K1_STEP(0, false);

    __builtin_amdgcn_s_barrier();
    if (SSM) {
        const int chan = cb * 64 + wn * 32 + i32;
        float hr = 0.f, hi = 0.f;
        const int tb = 64 * wm + 32 * h;
        const int odd = i32 & 1;
        const int cw = 64 * wn + 32 * odd + (i32 & ~1);
#pragma unroll
        for (int m = 0; m < 2; ++m)
#pragma unroll
            for (int r = 0; r < 16; ++r) {
                float ur = acc[m][0][r], ui = acc[m][1][r];
                float br = cr * ur - ci * ui, bi = cr * ui + ci * ur;
                float nr = lr * hr - li * hi + br;
                float ni = lr * hi + li * hr + bi;
                hr = nr; hi = ni;
                const float snd = odd ? hr : -hi;
                const float rcv = __builtin_bit_cast(float, __builtin_amdgcn_mov_dpp(__builtin_bit_cast(int, snd), 0xB1, 0xF, 0xF, true));
                const unsigned pk = odd ? pack2(rcv, -hi) : pack2(hr, rcv);
                const int t = tb + 16 * m + r;
                *(unsigned*)(smem + t * 256 + (((cw >> 3) ^ ((t >> 2) & 15)) << 4) + (cw & 7) * 2) = pk;
            }
        float e0r = __shfl(hr, i32), e0i = __shfl(hi, i32);
        const int chunk = (row0 >> 6) + wm;
        float pr = lr, pi = li;
#pragma unroll
        for (int s = 0; s < 5; ++s) csq(pr, pi);
        if (h == 0) {
            a.e32[chunk * P_ + chan] = make_float2(hr, hi);
        } else {
            *(f32x2*)(smem + 65536 + (wm * 64 + wn * 32 + i32) * 8) = (f32x2){hr + pr * e0r - pi * e0i, hi + pr * e0i + pi * e0r};
            csq(pr, pi);
            if (wm == 0) *(f32x2*)(smem + 65536 + 2048 + (wn * 32 + i32) * 8) = (f32x2){pr, pi};
        }
    } else {
        const int n0 = (cb - 8) * 128;
        u32x2 bb[2][4];
#pragma unroll
        for (int nn = 0; nn < 2; ++nn)
#pragma unroll
            for (int q = 0; q < 4; ++q) bb[nn][q] = *(const u32x2*)(a.b1 + n0 + 64 * wn + 32 * nn + 8 * q + 4 * h);
#pragma unroll
        for (int nn = 0; nn < 2; ++nn) {
#pragma unroll
            for (int q = 0; q < 4; ++q) {
                const int nc = 8 * wn + 4 * nn + q;
                float bv[4] = {bits2f(bb[nn][q][0] << 16), bits2f(bb[nn][q][0] & 0xffff0000u),
                               bits2f(bb[nn][q][1] << 16), bits2f(bb[nn][q][1] & 0xffff0000u)};
#pragma unroll
                for (int m = 0; m < 2; ++m) {
                    const int t = 64 * wm + 32 * ((i32 >> 2) & 1) + 16 * m + (i32 & 3) + 4 * (i32 >> 3);
                    f32x2 g0 = gelu2((f32x2){acc[m][nn][4 * q], acc[m][nn][4 * q + 1]} + (f32x2){bv[0], bv[1]});
                    f32x2 g1 = gelu2((f32x2){acc[m][nn][4 * q + 2], acc[m][nn][4 * q + 3]} + (f32x2){bv[2], bv[3]});
                    u32x2 o; o[0] = pack2(g0[0], g0[1]); o[1] = pack2(g1[0], g1[1]);
                    *(u32x2*)(smem + t * 256 + ((nc ^ ((t >> 2) & 15)) << 4) + 8 * h) = o;
                }
            }
        }
    }
    __syncthreads();
    if (SSM && tid < 64) {
        const f32x2 q = *(const f32x2*)(smem + 65536 + 2048 + tid * 8);
        const int chan = cb * 64 + tid;
        const f32x2 e0 = *(const f32x2*)(smem + 65536 + tid * 8);
        const f32x2 e1 = *(const f32x2*)(smem + 65536 + (64 + tid) * 8);
        a.e64[(rb * 2) * P_ + chan] = make_float2(0.f, 0.f);
        a.e64[(rb * 2 + 1) * P_ + chan] = make_float2(e0[0], e0[1]);
        a.e256[rb * P_ + chan] = make_float2(q[0] * e0[0] - q[1] * e0[1] + e1[0], q[0] * e0[1] + q[1] * e0[0] + e1[1]);
    }
    {
        const int i16 = lane & 15, g4 = lane >> 4;
#pragma unroll
        for (int it = 0; it < 8; ++it) {
            const int f = it * NW + wid;
            const int rtl = f >> 2, ksl = f & 3;
            const int t = 64 * (rtl >> 2) + 4 * i16 + (rtl & 3);
            u32x4 v = *(const u32x4*)(smem + t * 256 + (((4 * ksl + g4) ^ ((t >> 2) & 15)) << 4));
            const int rt = rb * (BM / 16) + rtl;
            if (SSM) {
                const int ks = (ksl & 1) * 16 + 2 * cb + (ksl >> 1);
                *(u32x4*)((char*)a.ha + ((size_t)(rt * 32 + ks) * 64 + lane) * 16) = v;
            } else {
                const int ks = (cb - 8) * 4 + ksl;
                *(u32x4*)((char*)a.hm + ((size_t)(rt * 56 + ks) * 64 + lane) * 16) = v;
            }
        }
    }
}

__global__ __launch_bounds__(512, 2) void k1_kernel(K1Args a) {
    __shared__ __attribute__((aligned(16))) char smem[3 * STG];
    const int bid = blockIdx.x;
    const int tid = threadIdx.x, wid = tid >> 6, lane = tid & 63;
    const int xcd = bid & 7, idx = bid >> 3;
    if (idx >= 16) {
        k1_gemm<false>(a, smem, xcd, 8 + (idx - 16));
        return;
    }
    if (wid < 4) {
        k1_gemm<true>(a, smem, 2 * xcd + (idx & 1), idx >> 1);
        return;
    }
    const int u = (xcd * 16 + idx) * 4 + (wid - 4);
    const int i16 = lane & 15, g4 = lane >> 4;
    const u32x4 gv = *(const u32x4*)(a.gw + lane * 8);
    u32x4 xv[4];
#pragma unroll
    for (int r = 0; r < 4; ++r) xv[r] = *(const u32x4*)(a.x + (size_t)(4 * u + r) * D_ + lane * 8);
    u32x4 cv[2];
#pragma unroll
    for (int j = 0; j < 2; ++j) {
        const int f = 2 * u + j, nt = f >> 5, ks = f & 31;
        cv[j] = *(const u32x4*)((ks < 16 ? a.Cre : a.Cim) + (size_t)(16 * nt + i16) * P_ + 32 * (ks & 15) + 8 * g4);
    }
    if (u < 448) {
        unsigned short wv[4][8];
#pragma unroll
        for (int j = 0; j < 4; ++j) {
            const int f = 4 * u + j, nt = f / 56, ks = f - nt * 56;
            const unsigned short* src = (const unsigned short*)a.w2 + (size_t)(32 * ks + 8 * g4) * D_ + 16 * nt + i16;
#pragma unroll
            for (int e = 0; e < 8; ++e) wv[j][e] = src[e * D_];
        }
#pragma unroll
        for (int j = 0; j < 4; ++j) {
            u32x4 o;
#pragma unroll
            for (int e = 0; e < 4; ++e) o[e] = (unsigned)wv[j][2 * e] | ((unsigned)wv[j][2 * e + 1] << 16);
            *(u32x4*)((char*)a.w2t + ((size_t)(4 * u + j) * 64 + lane) * 16) = o;
        }
    }
#pragma unroll
    for (int j = 0; j < 2; ++j) *(u32x4*)((char*)a.cf + ((size_t)(2 * u + j) * 64 + lane) * 16) = cv[j];
    {
        float gwf[8];
#pragma unroll
        for (int e = 0; e < 4; ++e) { gwf[2 * e] = bits2f(gv[e] << 16); gwf[2 * e + 1] = bits2f(gv[e] & 0xffff0000u); }
        float mine = 0.f;
#pragma unroll
        for (int r = 0; r < 4; ++r) {
            float sacc = 0.f;
#pragma unroll
            for (int e = 0; e < 4; ++e) {
                sacc += rbf(bits2f(xv[r][e] << 16) * gwf[2 * e]);
                sacc += rbf(bits2f(xv[r][e] & 0xffff0000u) * gwf[2 * e + 1]);
            }
#pragma unroll
            for (int o = 32; o > 0; o >>= 1) sacc += __shfl_xor(sacc, o);
            if (lane == r) mine = sacc;
        }
        if (lane < 4) {
            float gs = rbf(mine);
            a.gate[4 * u + lane] = rbf(1.0f / (1.0f + expf(-gs)));
        }
    }
    if (u < 8) {
        const int p = u * 64 + lane;
        float lr, li, t0, t1;
        ssm_params(a.lre, a.lim, a.logdt, p, lr, li, t0, t1);
        a.lam[p] = make_float2(lr, li);
        csq(lr, li); csq(lr, li);
        a.lam[512 + p] = make_float2(lr, li);
        csq(lr, li); csq(lr, li); csq(lr, li);
        a.lam[1024 + p] = make_float2(lr, li);
        csq(lr, li);
        a.lam[1536 + p] = make_float2(lr, li);
        csq(lr, li);
        a.lam[2048 + p] = make_float2(lr, li);
        a.e256[p - P_] = make_float2(0.f, 0.f);
    }
}

struct K2Args {
    const bf16* x; const bf16* cf;
    const bf16* Dsk; const bf16* b2;
    const bf16* ha; const bf16* hm; const bf16* w2t; const float2* e64; const float2* e32; const float* gate;
    const float2* lam; const float2* e256;
    float* out;
};

#define ZS_STRIDE 4112
#define LAM_OFF (16 * ZS_STRIDE)

#define K2_SETUP_LOADS()                                                                             \
    f32x2 ev[15];                                                                                    \
    {                                                                                                \
        const f32x2* ep = (const f32x2*)a.e256 + tid;                                                \
        _Pragma("unroll") for (int i = 0; i < 15; ++i) {                                             \
            int j = (rb >> 1) - 1 - i; j = j < -1 ? -1 : j;                                          \
            ev[i] = ep[j * P_];                                                                      \
        }                                                                                            \
    }                                                                                                \
    const f32x2 pfx = *((const f32x2*)a.e64 + (size_t)rb * P_ + tid);                                \
    const f32x2 e32v = *((const f32x2*)a.e32 + (size_t)rb * P_ + tid);                               \
    const f32x2 l1 = *((const f32x2*)a.lam + tid);                                                   \
    const f32x2 l4 = *((const f32x2*)a.lam + 512 + tid);                                             \
    const f32x2 l32 = *((const f32x2*)a.lam + 1024 + tid);                                           \
    const f32x2 l64 = *((const f32x2*)a.lam + 1536 + tid);                                           \
    const f32x2 l256 = *((const f32x2*)a.lam + 2048 + tid);
#define K2_SETUP_COMPUTE()                                                                           \
    {                                                                                                \
        float Cr = 0.f, Ci = 0.f;                                                                    \
        _Pragma("unroll") for (int i = 14; i >= 0; --i) {                                            \
            float nr = l256[0] * Cr - l256[1] * Ci + ev[i][0];                                       \
            float ni = l256[0] * Ci + l256[1] * Cr + ev[i][1];                                       \
            Cr = nr; Ci = ni;                                                                        \
        }                                                                                            \
        _Pragma("unroll") for (int i = 0; i < 1; ++i) {                                              \
            float nr = l64[0] * Cr - l64[1] * Ci;                                                    \
            float ni = l64[0] * Ci + l64[1] * Cr;                                                    \
            const bool u = i < (rb & 1);                                                             \
            Cr = u ? nr : Cr; Ci = u ? ni : Ci;                                                      \
        }                                                                                            \
        Cr += pfx[0]; Ci += pfx[1];                                                                  \
        const float c1r = e32v[0] + l32[0] * Cr - l32[1] * Ci;                                       \
        const float c1i = e32v[1] + l32[0] * Ci + l32[1] * Cr;                                       \
        _Pragma("unroll") for (int sub = 0; sub < 2; ++sub) {                                        \
            const float cr_ = sub ? c1r : Cr, ci_ = sub ? c1i : Ci;                                  \
            float zr = cr_ * l1[0] - ci_ * l1[1];                                                    \
            float zi = cr_ * l1[1] + ci_ * l1[0];                                                    \
            _Pragma("unroll") for (int ii = 0; ii < 8; ++ii) {                                       \
                *(f32x2*)(smem + (sub * 8 + ii) * ZS_STRIDE + tid * 8) = (f32x2){zr, zi};            \
                float nr = zr * l4[0] - zi * l4[1];                                                  \
                float ni = zr * l4[1] + zi * l4[0];                                                  \
                zr = nr; zi = ni;                                                                    \
            }                                                                                        \
        }                                                                                            \
        *(f32x2*)(smem + LAM_OFF + tid * 8) = l1;                                                    \
    }                                                                                                \
    asm volatile("s_waitcnt lgkmcnt(0)" ::: "memory");                                               \
    __builtin_amdgcn_s_barrier();

__global__ __launch_bounds__(512, 2) void k2_kernel(K2Args a) {
    __shared__ __attribute__((aligned(16))) char smem[131072];
    const int bid = blockIdx.x;
    const int tid = threadIdx.x, wid = tid >> 6, lane = tid & 63;
    const int idx = bid >> 3;
    const int rb = (bid & 7) * 4 + (idx >> 3), cbk = idx & 7;
    const int row0 = rb * 64, col0 = cbk * 64;
    const int i16 = lane & 15, g4 = lane >> 4;
    const int widu = __builtin_amdgcn_readfirstlane(wid);
    const int voff = lane * 16;

    f32x4 acc[4][4];
#pragma unroll
    for (int m = 0; m < 4; ++m)
#pragma unroll
        for (int n = 0; n < 4; ++n) acc[m][n] = (f32x4){0.f, 0.f, 0.f, 0.f};

    if (widu < 4) {
        const __amdgpu_buffer_rsrc_t ra = __builtin_amdgcn_make_buffer_rsrc((void*)a.hm, 0, 128 * 56 * 1024, 0x00020000);
        const __amdgpu_buffer_rsrc_t rbs = __builtin_amdgcn_make_buffer_rsrc((void*)a.w2t, 0, 32 * 56 * 1024, 0x00020000);
        const int abase = (rb * 4 * 56 + widu * 14) * 1024, bbase = (cbk * 4 * 56 + widu * 14) * 1024;
        bf16x8 A[4][4], B[4][4];
#define K2LD(bi, s)                                                                                  \
        do {                                                                                         \
            _Pragma("unroll") for (int m = 0; m < 4; ++m)                                            \
                A[bi][m] = __builtin_bit_cast(bf16x8, __builtin_amdgcn_raw_buffer_load_b128(ra, voff, abase + (m * 56 + (s)) * 1024, 0)); \
            _Pragma("unroll") for (int n = 0; n < 4; ++n)                                            \
                B[bi][n] = __builtin_bit_cast(bf16x8, __builtin_amdgcn_raw_buffer_load_b128(rbs, voff, bbase + (n * 56 + (s)) * 1024, 0)); \
        } while (0)
#define K2MM(bi)                                                                                     \
        do {                                                                                         \
            _Pragma("unroll") for (int m = 0; m < 4; ++m)                                            \
                _Pragma("unroll") for (int n = 0; n < 4; ++n)                                        \
                    acc[m][n] = __builtin_amdgcn_mfma_f32_16x16x32_bf16(A[bi][m], B[bi][n], acc[m][n], 0, 0, 0); \
        } while (0)
        K2_SETUP_LOADS();
        SB;
        K2LD(0, 0); K2LD(1, 1); K2LD(2, 2);
        SB;
        K2_SETUP_COMPUTE();
        SB;
#pragma unroll
        for (int s = 0; s < 14; ++s) {
            SB;
            if (s + 3 < 14) K2LD((s + 3) % 4, s + 3);
            SB;
            K2MM(s % 4);
        }
        SB;
    } else {
        const int wz = widu - 4;
        const __amdgpu_buffer_rsrc_t ra = __builtin_amdgcn_make_buffer_rsrc((void*)a.ha, 0, 128 * 32 * 1024, 0x00020000);
        const __amdgpu_buffer_rsrc_t rbs = __builtin_amdgcn_make_buffer_rsrc((void*)a.cf, 0, 32 * 32 * 1024, 0x00020000);
        const int abase = (rb * 4 * 32 + wz * 4) * 1024, bbase = (cbk * 4 * 32 + wz * 4) * 1024;
        bf16x8 Hr[4], Hi[4], Br[2][4], Bi[2][4];
#define HLD(sl)                                                                                      \
        do {                                                                                         \
            _Pragma("unroll") for (int m = 0; m < 4; ++m) {                                          \
                Hr[m] = __builtin_bit_cast(bf16x8, __builtin_amdgcn_raw_buffer_load_b128(ra, voff, abase + (m * 32 + (sl)) * 1024, 0)); \
                Hi[m] = __builtin_bit_cast(bf16x8, __builtin_amdgcn_raw_buffer_load_b128(ra, voff, abase + (m * 32 + 16 + (sl)) * 1024, 0)); \
            }                                                                                        \
        } while (0)
#define ZLD(bi, sl)                                                                                  \
        do {                                                                                         \
            _Pragma("unroll") for (int n = 0; n < 4; ++n) {                                          \
                Br[bi][n] = __builtin_bit_cast(bf16x8, __builtin_amdgcn_raw_buffer_load_b128(rbs, voff, bbase + (n * 32 + (sl)) * 1024, 0)); \
                Bi[bi][n] = __builtin_bit_cast(bf16x8, __builtin_amdgcn_raw_buffer_load_b128(rbs, voff, bbase + (n * 32 + 16 + (sl)) * 1024, 0)); \
            }                                                                                        \
        } while (0)
        K2_SETUP_LOADS();
        SB;
        HLD(0); ZLD(0, 0); ZLD(1, 1);
        SB;
        K2_SETUP_COMPUTE();
        SB;
        const char* zt = smem + i16 * ZS_STRIDE + (128 * wz + 8 * g4) * 8;
        const char* lt = smem + LAM_OFF + (128 * wz + 8 * g4) * 8;
#pragma unroll
        for (int sl = 0; sl < 4; ++sl) {
            const int cur = sl & 1;
            SB;
#pragma unroll
            for (int m = 0; m < 4; ++m)
#pragma unroll
                for (int n = 0; n < 4; ++n) {
                    acc[m][n] = __builtin_amdgcn_mfma_f32_16x16x32_bf16(Hr[m], Br[cur][n], acc[m][n], 0, 0, 0);
                    acc[m][n] = __builtin_amdgcn_mfma_f32_16x16x32_bf16(Hi[m], Bi[cur][n], acc[m][n], 0, 0, 0);
                }
            SB;
            if (sl + 1 < 4) HLD(sl + 1);
            SB;
            f32x4 z[4], lm[4];
#pragma unroll
            for (int q = 0; q < 4; ++q) {
                z[q] = *(const f32x4*)(zt + sl * 256 + q * 16);
                lm[q] = *(const f32x4*)(lt + sl * 256 + q * 16);
            }
#pragma unroll
            for (int m = 0; m < 4; ++m) {
                u32x4 zr, zi;
#pragma unroll
                for (int q = 0; q < 4; ++q) {
                    zr[q] = pack2(z[q][0], z[q][2]);
                    zi[q] = pack2(-z[q][1], -z[q][3]);
                    if (m < 3) {
                        float nr0 = z[q][0] * lm[q][0] - z[q][1] * lm[q][1];
                        float ni0 = z[q][0] * lm[q][1] + z[q][1] * lm[q][0];
                        float nr1 = z[q][2] * lm[q][2] - z[q][3] * lm[q][3];
                        float ni1 = z[q][2] * lm[q][3] + z[q][3] * lm[q][2];
                        z[q] = (f32x4){nr0, ni0, nr1, ni1};
                    }
                }
                bf16x8 Zr = __builtin_bit_cast(bf16x8, zr), Zi = __builtin_bit_cast(bf16x8, zi);
#pragma unroll
                for (int n = 0; n < 4; ++n) {
                    acc[m][n] = __builtin_amdgcn_mfma_f32_16x16x32_bf16(Zr, Br[cur][n], acc[m][n], 0, 0, 0);
                    acc[m][n] = __builtin_amdgcn_mfma_f32_16x16x32_bf16(Zi, Bi[cur][n], acc[m][n], 0, 0, 0);
                }
            }
            SB;
            if (sl + 2 < 4) ZLD(cur, sl + 2);
        }
        SB;
    }
    float gv[2][4], xv[2][4], dsk[2], bb[2];
#pragma unroll
    for (int f = 0; f < 2; ++f) {
        const int fr = wid * 2 + f;
        const int m = fr >> 2, n = fr & 3;
        const int d = col0 + 16 * n + i16;
        dsk[f] = b2f(a.Dsk[d]); bb[f] = b2f(a.b2[d]);
#pragma unroll
        for (int r = 0; r < 4; ++r) {
            const int t = row0 + 16 * g4 + 4 * r + m;
            gv[f][r] = a.gate[t];
            xv[f][r] = b2f(a.x[(size_t)t * D_ + d]);
        }
    }
    __syncthreads();
#pragma unroll
    for (int m = 0; m < 4; ++m)
#pragma unroll
        for (int n = 0; n < 4; ++n)
            *(f32x4*)(smem + ((wid * 16 + m * 4 + n) * 64 + lane) * 16) = acc[m][n];
    __syncthreads();
#pragma unroll
    for (int f = 0; f < 2; ++f) {
        const int fr = wid * 2 + f;
        const int m = fr >> 2, n = fr & 3;
        f32x4 sm_ = (f32x4){0.f, 0.f, 0.f, 0.f}, ss = (f32x4){0.f, 0.f, 0.f, 0.f};
#pragma unroll
        for (int w = 0; w < 4; ++w) {
            sm_ += *(const f32x4*)(smem + ((w * 16 + fr) * 64 + lane) * 16);
            ss += *(const f32x4*)(smem + (((w + 4) * 16 + fr) * 64 + lane) * 16);
        }
        const int d = col0 + 16 * n + i16;
#pragma unroll
        for (int r = 0; r < 4; ++r) {
            const int t = row0 + 16 * g4 + 4 * r + m;
            const float g = gv[f][r];
            const float av = 2.0f * ss[r] + rbf(xv[f][r] * dsk[f]);
            const float bv = rbf(rbf(sm_[r]) + bb[f]);
            a.out[(size_t)t * D_ + d] = g * av + rbf(rbf(1.0f - g) * bv);
        }
    }
}

extern "C" void kernel_launch(void* const* d_in, const int* in_sizes, int n_in,
                              void* d_out, int out_size, void* d_ws, size_t ws_size,
                              hipStream_t stream) {
    char* ws = (char*)d_ws;
    K1Args a1;
    a1.x = (const bf16*)d_in[0]; a1.lre = (const bf16*)d_in[1]; a1.lim = (const bf16*)d_in[2];
    a1.Bre = (const bf16*)d_in[3]; a1.Bim = (const bf16*)d_in[4]; a1.logdt = (const bf16*)d_in[7];
    a1.w1 = (const bf16*)d_in[10]; a1.b1 = (const bf16*)d_in[11]; a1.w2 = (const bf16*)d_in[12];
    a1.gw = (const bf16*)d_in[14];
    a1.Cre = (const bf16*)d_in[5]; a1.Cim = (const bf16*)d_in[6]; a1.cf = (bf16*)(ws + WS_CF);
    a1.ha = (bf16*)(ws + WS_HA); a1.hm = (bf16*)(ws + WS_HM); a1.w2t = (bf16*)(ws + WS_W2T);
    a1.e64 = (float2*)(ws + WS_E64); a1.e32 = (float2*)(ws + WS_E32); a1.gate = (float*)(ws + WS_G);
    a1.lam = (float2*)(ws + WS_LAM); a1.e256 = (float2*)(ws + WS_E256);
    K2Args a2;
    a2.x = a1.x;
    a2.cf = a1.cf; a2.Dsk = (const bf16*)d_in[8];
    a2.b2 = (const bf16*)d_in[13];
    a2.ha = a1.ha; a2.hm = a1.hm; a2.w2t = a1.w2t; a2.e64 = a1.e64; a2.e32 = a1.e32; a2.gate = a1.gate;
    a2.lam = a1.lam; a2.e256 = a1.e256;
    a2.out = (float*)d_out;
    k1_kernel<<<240, 512, 0, stream>>>(a1);
    k2_kernel<<<256, 512, 0, stream>>>(a2);
}
```

```cpp
#include <hip/hip_runtime.h>
#include <hip/hip_bf16.h>
#include <stdint.h>

typedef __hip_bfloat16 bf16;
typedef __attribute__((ext_vector_type(8))) short bf16x8;
typedef __attribute__((ext_vector_type(4))) short s16x4;
typedef __attribute__((ext_vector_type(4))) float f32x4;
typedef __attribute__((ext_vector_type(2))) float f32x2;
typedef __attribute__((ext_vector_type(16))) float f32x16;
typedef __attribute__((ext_vector_type(4))) unsigned u32x4;
typedef __attribute__((ext_vector_type(2))) unsigned u32x2;

#define L_ 2048
#define D_ 512
#define P_ 512
#define F_ 1792

#define WS_HA   0ull
#define WS_CF   (4ull << 20)
#define WS_HM   (8ull << 20)
#define WS_W2T  (16ull << 20)
#define WS_E64  ((18ull << 20) + 4096)
#define WS_E32  ((18ull << 20) + 139264)
#define WS_G    ((18ull << 20) + 270336)
#define WS_LAM  ((18ull << 20) + 280576)
#define WS_E256 ((18ull << 20) + 307200)

#define SB __builtin_amdgcn_sched_barrier(0)

__device__ __forceinline__ float bits2f(unsigned u) { return __uint_as_float(u); }
__device__ __forceinline__ float b2f(bf16 v) { return __bfloat162float(v); }
typedef __attribute__((ext_vector_type(2))) __bf16 nbf16x2;
__device__ __forceinline__ unsigned pack2(float lo, float hi) {
    nbf16x2 v = {(__bf16)lo, (__bf16)hi};
    return __builtin_bit_cast(unsigned, v);
}
__device__ __forceinline__ float rbf(float v) { return bits2f(pack2(v, v) << 16); }

__device__ __forceinline__ void glds16(const void* src, void* lds) {
    __builtin_amdgcn_global_load_lds((const unsigned*)src, (unsigned*)lds, 16, 0, 0);
}

__device__ __forceinline__ void ssm_params(const bf16* lre, const bf16* lim, const bf16* logdt, int p,
                                           float& br, float& bi, float& cr, float& ci) {
    float lr = b2f(lre[p]), li = b2f(lim[p]);
    float dt = rbf(expf(b2f(logdt[p])));
    float ar = dt * lr, ai = dt * li;
    float e = expf(ar);
    float s, c;
    sincosf(ai, &s, &c);
    br = e * c; bi = e * s;
    float nr = br - 1.0f, ni = bi;
    float den = lr * lr + li * li;
    cr = (nr * lr + ni * li) / den;
    ci = (ni * lr - nr * li) / den;
}
__device__ __forceinline__ void csq(float& r, float& i) { float a = r * r - i * i, b = 2.0f * r * i; r = a; i = b; }

__device__ __forceinline__ f32x2 gelu2(f32x2 v) {
    const float c0 = -2.0f * 1.4426950408889634f * 0.7978845608028654f;
    const float c1 = c0 * 0.044715f;
    f32x2 t = (v * v) * c1 + c0;
    f32x2 a2 = v * t;
    f32x2 d = (f32x2){__builtin_amdgcn_exp2f(a2[0]), __builtin_amdgcn_exp2f(a2[1])} + 1.0f;
    return v * (f32x2){__builtin_amdgcn_rcpf(d[0]), __builtin_amdgcn_rcpf(d[1])};
}

#define STG 49152

struct K1Args {
    const bf16* x; const bf16* lre; const bf16* lim; const bf16* Bre; const bf16* Bim; const bf16* logdt;
    const bf16* w1; const bf16* b1; const bf16* w2; const bf16* gw; const bf16* Cre; const bf16* Cim;
    bf16* cf; bf16* ha; bf16* hm; bf16* w2t; float2* e64; float2* e32; float* gate; float2* lam; float2* e256;
};

template <bool SSM>
__device__ __forceinline__ void k1_gemm(const K1Args& a, char* smem, int rb, int cb) {
    const int tid = threadIdx.x, wid = tid >> 6, lane = tid & 63;
    constexpr int NW = SSM ? 4 : 8;
    constexpr int BM = SSM ? 128 : 256;
    constexpr int NB = SSM ? 4 : 2;
    constexpr int BOFF = BM * 128;
    const int wm = SSM ? (wid & 1) : (wid & 3), wn = SSM ? (wid >> 1) : (wid >> 2);
    const int row0 = rb * BM;
    const int h = lane >> 5, i32 = lane & 31;

    const bf16* asrc[4];
    const bf16* bsrc[NB];
#pragma unroll
    for (int i = 0; i < 4; ++i) {
        int j = wid + NW * i;
        int row = 8 * j + (lane >> 3);
        int ch = (lane & 7) ^ ((row >> 1) & 7);
        asrc[i] = a.x + (size_t)(row0 + row) * D_ + ch * 8;
    }
#pragma unroll
    for (int i = 0; i < NB; ++i) {
        int j = wid + NW * i;
        if (SSM) {
            int n = 8 * j + (lane >> 3);
            int ch = (lane & 7) ^ ((n >> 1) & 7);
            int chan = cb * 64 + (n >> 6) * 32 + (n & 31);
            const bf16* base = ((n >> 5) & 1) ? a.Bim : a.Bre;
            bsrc[i] = base + (size_t)chan * D_ + ch * 8;
        } else {
            int k = 4 * j + (lane >> 4);
            int ch = (lane & 15) ^ (((k & 3) << 2) | ((k >> 2) & 3));
            bsrc[i] = a.w1 + (size_t)k * F_ + (cb - 8) * 128 + ch * 8;
        }
    }
    const int bstep = SSM ? 64 : 64 * F_;

    auto stage = [&](int kt, int buf) {
        char* base = smem + buf * STG;
#pragma unroll
        for (int i = 0; i < 4; ++i) glds16(asrc[i] + kt * 64, base + (wid + NW * i) * 1024 + lane * 16);
#pragma unroll
        for (int i = 0; i < NB; ++i) glds16(bsrc[i] + (size_t)kt * bstep, base + BOFF + (wid + NW * i) * 1024 + lane * 16);
    };

    const unsigned lds0 = (unsigned)(size_t)smem;
    unsigned aaddr[2][4];
#pragma unroll
    for (int m = 0; m < 2; ++m) {
        int row = 64 * wm + 32 * ((i32 >> 2) & 1) + 16 * m + (i32 & 3) + 4 * (i32 >> 3);
        int sw = (row >> 1) & 7;
#pragma unroll
        for (int ks = 0; ks < 4; ++ks) aaddr[m][ks] = lds0 + row * 128 + (((2 * ks + h) ^ sw) << 4);
    }
    unsigned baddr[2][4];
#pragma unroll
    for (int nn = 0; nn < 2; ++nn) {
        if (SSM) {
            int row = 64 * wn + 32 * nn + i32;
            int sw = (row >> 1) & 7;
#pragma unroll
            for (int ks = 0; ks < 4; ++ks) baddr[nn][ks] = lds0 + BOFF + row * 128 + (((2 * ks + h) ^ sw) << 4);
        } else {
            const int blk = (lane >> 4) & 1, q = (lane & 15) >> 2, p = lane & 3;
            const int c = 2 * wn + nn;
#pragma unroll
            for (int t = 0; t < 2; ++t) {
                int row = 8 * h + 4 * t + q;
                int ch = 4 * c + 2 * blk + (p >> 1);
                baddr[nn][t] = lds0 + BOFF + 256 * row + 16 * (ch ^ (((row & 3) << 2) | ((row >> 2) & 3))) + 8 * (p & 1);
            }
            baddr[nn][2] = baddr[nn][3] = 0;
        }
    }

    f32x16 acc[2][2];
#pragma unroll
    for (int m = 0; m < 2; ++m)
#pragma unroll
        for (int nn = 0; nn < 2; ++nn)
#pragma unroll
            for (int r = 0; r < 16; ++r) acc[m][nn][r] = 0.f;

    bf16x8 fa[2][2], fb[2][2];
#define RD_FRAG(s, ks)                                                                                        \
    do {                                                                                                      \
        _Pragma("unroll") for (int m = 0; m < 2; ++m)                                                         \
            asm volatile("ds_read_b128 %0, %1" : "=v"(fa[s][m]) : "v"(aaddr[m][ks] + bo));                    \
        if (SSM) {                                                                                            \
            _Pragma("unroll") for (int nn = 0; nn < 2; ++nn)                                                  \
                asm volatile("ds_read_b128 %0, %1" : "=v"(fb[s][nn]) : "v"(baddr[nn][ks] + bo));              \
        } else {                                                                                              \
            _Pragma("unroll") for (int nn = 0; nn < 2; ++nn) {                                                \
                s16x4 lo_, hi_;                                                                               \
                asm volatile("ds_read_b64_tr_b16 %0, %1 offset:%2" : "=v"(lo_) : "v"(baddr[nn][0] + bo), "i"((ks) * 4096)); \
                asm volatile("ds_read_b64_tr_b16 %0, %1 offset:%2" : "=v"(hi_) : "v"(baddr[nn][1] + bo), "i"((ks) * 4096)); \
                fb[s][nn] = __builtin_shufflevector(lo_, hi_, 0, 1, 2, 3, 4, 5, 6, 7);                        \
            }                                                                                                 \
        }                                                                                                     \
    } while (0)
#define WAIT_FRAG(s, N)                                                                                       \
    asm volatile("s_waitcnt lgkmcnt(" #N ")" : "+v"(fa[s][0]), "+v"(fa[s][1]), "+v"(fb[s][0]), "+v"(fb[s][1]))
#define MM_FRAG(s)                                                                                            \
    do {                                                                                                      \
        _Pragma("unroll") for (int m = 0; m < 2; ++m)                                                         \
            _Pragma("unroll") for (int nn = 0; nn < 2; ++nn) {                                                \
                if (SSM) acc[m][nn] = __builtin_amdgcn_mfma_f32_32x32x16_bf16(fa[s][m], fb[s][nn], acc[m][nn], 0, 0, 0); \
                else     acc[m][nn] = __builtin_amdgcn_mfma_f32_32x32x16_bf16(fb[s][nn], fa[s][m], acc[m][nn], 0, 0, 0); \
            }                                                                                                 \
    } while (0)

    stage(0, 0);
    stage(1, 1);
    float lr = 0.f, li = 0.f, cr = 0.f, ci = 0.f;
    if (SSM) ssm_params(a.lre, a.lim, a.logdt, cb * 64 + wn * 32 + i32, lr, li, cr, ci);
#define K1_STEP(WAITN, DOSTAGE)                                                                            \
    do {                                                                                                      \
        if (WAITN == 0) asm volatile("s_waitcnt vmcnt(0)" ::: "memory");                                      \
        else if (SSM) asm volatile("s_waitcnt vmcnt(8)" ::: "memory");                                        \
        else asm volatile("s_waitcnt vmcnt(6)" ::: "memory");                                                 \
        __builtin_amdgcn_s_barrier();                                                                         \
        RD_FRAG(0, 0);                                                                                        \
        if (DOSTAGE) stage(kt + 2, nbuf);                                                                     \
        SB;                                                                                                   \
        RD_FRAG(1, 1);                                                                                        \
        if (SSM) { WAIT_FRAG(0, 4); } else { WAIT_FRAG(0, 6); }                                               \
        SB; MM_FRAG(0); SB;                                                                                   \
        RD_FRAG(0, 2);                                                                                        \
        if (SSM) { WAIT_FRAG(1, 4); } else { WAIT_FRAG(1, 6); }                                               \
        SB; MM_FRAG(1); SB;                                                                                   \
        RD_FRAG(1, 3);                                                                                        \
        if (SSM) { WAIT_FRAG(0, 4); } else { WAIT_FRAG(0, 6); }                                               \
        SB; MM_FRAG(0); SB;                                                                                   \
        WAIT_FRAG(1, 0);                                                                                      \
        SB; MM_FRAG(1); SB;                                                                                   \
    } while (0)
    int bo = 0, nbuf = 2;
    int kt = 0;
#pragma unroll 1
    for (; kt < 6; ++kt) {
        K1_STEP(6, true);
        bo = (bo == 2 * STG) ? 0 : bo + STG;
        nbuf = (nbuf == 2) ? 0 : nbuf + 1;
    }
    K1_STEP(6, false);
    bo = (bo == 2 * STG) ? 0 : bo + STG;
    K1_STEP(0, false);

    __builtin_amdgcn_s_barrier();
    if (SSM) {
        const int chan = cb * 64 + wn * 32 + i32;
        float hr = 0.f, hi = 0.f;
        const int tb = 64 * wm + 32 * h;
        const int odd = i32 & 1;
        const int cw = 64 * wn + 32 * odd + (i32 & ~1);
#pragma unroll
        for (int m = 0; m < 2; ++m)
#pragma unroll
            for (int r = 0; r < 16; ++r) {
                float ur = acc[m][0][r], ui = acc[m][1][r];
                float br = cr * ur - ci * ui, bi = cr * ui + ci * ur;
                float nr = lr * hr - li * hi + br;
                float ni = lr * hi + li * hr + bi;
                hr = nr; hi = ni;
                const float snd = odd ? hr : -hi;
                const float rcv = __builtin_bit_cast(float, __builtin_amdgcn_mov_dpp(__builtin_bit_cast(int, snd), 0xB1, 0xF, 0xF, true));
                const unsigned pk = odd ? pack2(rcv, -hi) : pack2(hr, rcv);
                const int t = tb + 16 * m + r;
                *(unsigned*)(smem + t * 256 + (((cw >> 3) ^ ((t >> 2) & 15)) << 4) + (cw & 7) * 2) = pk;
            }
        float e0r = __shfl(hr, i32), e0i = __shfl(hi, i32);
        const int chunk = (row0 >> 6) + wm;
        float pr = lr, pi = li;
#pragma unroll
        for (int s = 0; s < 5; ++s) csq(pr, pi);
        if (h == 0) {
            a.e32[chunk * P_ + chan] = make_float2(hr, hi);
        } else {
            *(f32x2*)(smem + 65536 + (wm * 64 + wn * 32 + i32) * 8) = (f32x2){hr + pr * e0r - pi * e0i, hi + pr * e0i + pi * e0r};
            csq(pr, pi);
            if (wm == 0) *(f32x2*)(smem + 65536 + 2048 + (wn * 32 + i32) * 8) = (f32x2){pr, pi};
        }
    } else {
        const int n0 = (cb - 8) * 128;
        u32x2 bb[2][4];
#pragma unroll
        for (int nn = 0; nn < 2; ++nn)
#pragma unroll
            for (int q = 0; q < 4; ++q) bb[nn][q] = *(const u32x2*)(a.b1 + n0 + 64 * wn + 32 * nn + 8 * q + 4 * h);
#pragma unroll
        for (int nn = 0; nn < 2; ++nn) {
#pragma unroll
            for (int q = 0; q < 4; ++q) {
                const int nc = 8 * wn + 4 * nn + q;
                float bv[4] = {bits2f(bb[nn][q][0] << 16), bits2f(bb[nn][q][0] & 0xffff0000u),
                               bits2f(bb[nn][q][1] << 16), bits2f(bb[nn][q][1] & 0xffff0000u)};
#pragma unroll
                for (int m = 0; m < 2; ++m) {
                    const int t = 64 * wm + 32 * ((i32 >> 2) & 1) + 16 * m + (i32 & 3) + 4 * (i32 >> 3);
                    f32x2 g0 = gelu2((f32x2){acc[m][nn][4 * q], acc[m][nn][4 * q + 1]} + (f32x2){bv[0], bv[1]});
                    f32x2 g1 = gelu2((f32x2){acc[m][nn][4 * q + 2], acc[m][nn][4 * q + 3]} + (f32x2){bv[2], bv[3]});
                    u32x2 o; o[0] = pack2(g0[0], g0[1]); o[1] = pack2(g1[0], g1[1]);
                    *(u32x2*)(smem + t * 256 + ((nc ^ ((t >> 2) & 15)) << 4) + 8 * h) = o;
                }
            }
        }
    }
    __syncthreads();
    if (SSM && tid < 64) {
        const f32x2 q = *(const f32x2*)(smem + 65536 + 2048 + tid * 8);
        const int chan = cb * 64 + tid;
        const f32x2 e0 = *(const f32x2*)(smem + 65536 + tid * 8);
        const f32x2 e1 = *(const f32x2*)(smem + 65536 + (64 + tid) * 8);
        a.e64[(rb * 2) * P_ + chan] = make_float2(0.f, 0.f);
        a.e64[(rb * 2 + 1) * P_ + chan] = make_float2(e0[0], e0[1]);
        a.e256[rb * P_ + chan] = make_float2(q[0] * e0[0] - q[1] * e0[1] + e1[0], q[0] * e0[1] + q[1] * e0[0] + e1[1]);
    }
    {
        const int i16 = lane & 15, g4 = lane >> 4;
#pragma unroll
        for (int it = 0; it < 8; ++it) {
            const int f = it * NW + wid;
            const int rtl = f >> 2, ksl = f & 3;
            const int t = 64 * (rtl >> 2) + 4 * i16 + (rtl & 3);
            u32x4 v = *(const u32x4*)(smem + t * 256 + (((4 * ksl + g4) ^ ((t >> 2) & 15)) << 4));
            const int rt = rb * (BM / 16) + rtl;
            if (SSM) {
                const int ks = (ksl & 1) * 16 + 2 * cb + (ksl >> 1);
                *(u32x4*)((char*)a.ha + ((size_t)(rt * 32 + ks) * 64 + lane) * 16) = v;
            } else {
                const int ks = (cb - 8) * 4 + ksl;
                *(u32x4*)((char*)a.hm + ((size_t)(rt * 56 + ks) * 64 + lane) * 16) = v;
            }
        }
    }
}

__global__ __launch_bounds__(512, 2) void k1_kernel(K1Args a) {
    __shared__ __attribute__((aligned(16))) char smem[3 * STG];
    const int bid = blockIdx.x;
    const int tid = threadIdx.x, wid = tid >> 6, lane = tid & 63;
    const int xcd = bid & 7, idx = bid >> 3;
    if (idx >= 16) {
        k1_gemm<false>(a, smem, xcd, 8 + (idx - 16));
        return;
    }
    if (wid < 4) {
        k1_gemm<true>(a, smem, 2 * xcd + (idx & 1), idx >> 1);
        return;
    }
    const int u = (xcd * 16 + idx) * 4 + (wid - 4);
    const int i16 = lane & 15, g4 = lane >> 4;
    const u32x4 gv = *(const u32x4*)(a.gw + lane * 8);
    u32x4 xv[4];
#pragma unroll
    for (int r = 0; r < 4; ++r) xv[r] = *(const u32x4*)(a.x + (size_t)(4 * u + r) * D_ + lane * 8);
    u32x4 cv[2];
#pragma unroll
    for (int j = 0; j < 2; ++j) {
        const int f = 2 * u + j, nt = f >> 5, ks = f & 31;
        cv[j] = *(const u32x4*)((ks < 16 ? a.Cre : a.Cim) + (size_t)(16 * nt + i16) * P_ + 32 * (ks & 15) + 8 * g4);
    }
    if (u < 448) {
        unsigned short wv[4][8];
#pragma unroll
        for (int j = 0; j < 4; ++j) {
            const int f = 4 * u + j, nt = f / 56, ks = f - nt * 56;
            const unsigned short* src = (const unsigned short*)a.w2 + (size_t)(32 * ks + 8 * g4) * D_ + 16 * nt + i16;
#pragma unroll
            for (int e = 0; e < 8; ++e) wv[j][e] = src[e * D_];
        }
#pragma unroll
        for (int j = 0; j < 4; ++j) {
            u32x4 o;
#pragma unroll
            for (int e = 0; e < 4; ++e) o[e] = (unsigned)wv[j][2 * e] | ((unsigned)wv[j][2 * e + 1] << 16);
            *(u32x4*)((char*)a.w2t + ((size_t)(4 * u + j) * 64 + lane) * 16) = o;
        }
    }
#pragma unroll
    for (int j = 0; j < 2; ++j) *(u32x4*)((char*)a.cf + ((size_t)(2 * u + j) * 64 + lane) * 16) = cv[j];
    {
        float gwf[8];
#pragma unroll
        for (int e = 0; e < 4; ++e) { gwf[2 * e] = bits2f(gv[e] << 16); gwf[2 * e + 1] = bits2f(gv[e] & 0xffff0000u); }
        float mine = 0.f;
#pragma unroll
        for (int r = 0; r < 4; ++r) {
            float sacc = 0.f;
#pragma unroll
            for (int e = 0; e < 4; ++e) {
                sacc += rbf(bits2f(xv[r][e] << 16) * gwf[2 * e]);
                sacc += rbf(bits2f(xv[r][e] & 0xffff0000u) * gwf[2 * e + 1]);
            }
#pragma unroll
            for (int o = 32; o > 0; o >>= 1) sacc += __shfl_xor(sacc, o);
            if (lane == r) mine = sacc;
        }
        if (lane < 4) {
            float gs = rbf(mine);
            a.gate[4 * u + lane] = rbf(1.0f / (1.0f + expf(-gs)));
        }
    }
    if (u < 8) {
        const int p = u * 64 + lane;
        float lr, li, t0, t1;
        ssm_params(a.lre, a.lim, a.logdt, p, lr, li, t0, t1);
        a.lam[p] = make_float2(lr, li);
        csq(lr, li); csq(lr, li);
        a.lam[512 + p] = make_float2(lr, li);
        csq(lr, li); csq(lr, li); csq(lr, li);
        a.lam[1024 + p] = make_float2(lr, li);
        csq(lr, li);
        a.lam[1536 + p] = make_float2(lr, li);
        csq(lr, li);
        a.lam[2048 + p] = make_float2(lr, li);
        a.e256[p - P_] = make_float2(0.f, 0.f);
    }
}

struct K2Args {
    const bf16* x; const bf16* cf;
    const bf16* Dsk; const bf16* b2;
    const bf16* ha; const bf16* hm; const bf16* w2t; const float2* e64; const float2* e32; const float* gate;
    const float2* lam; const float2* e256;
    float* out;
};

#define ZS_STRIDE 1040
#define LAM_OFF (16 * ZS_STRIDE)
#define SSM_TAB (LAM_OFF + 1024)

__global__ __launch_bounds__(512, 2) void k2_kernel(K2Args a) {
    __shared__ __attribute__((aligned(16))) char smem[131072];
    const int bid = blockIdx.x;
    const int tid = threadIdx.x, wid = tid >> 6, lane = tid & 63;
    const int idx = bid >> 3;
    const int rb = (bid & 7) * 4 + (idx >> 3), cbk = idx & 7;
    const int row0 = rb * 64, col0 = cbk * 64;
    const int i16 = lane & 15, g4 = lane >> 4;
    const int widu = __builtin_amdgcn_readfirstlane(wid);
    const int voff = lane * 16;

    f32x4 acc[4][4];
#pragma unroll
    for (int m = 0; m < 4; ++m)
#pragma unroll
        for (int n = 0; n < 4; ++n) acc[m][n] = (f32x4){0.f, 0.f, 0.f, 0.f};

    if (widu < 4) {
        const __amdgpu_buffer_rsrc_t ra = __builtin_amdgcn_make_buffer_rsrc((void*)a.hm, 0, 128 * 56 * 1024, 0x00020000);
        const __amdgpu_buffer_rsrc_t rbs = __builtin_amdgcn_make_buffer_rsrc((void*)a.w2t, 0, 32 * 56 * 1024, 0x00020000);
        const int abase = (rb * 4 * 56 + widu * 14) * 1024, bbase = (cbk * 4 * 56 + widu * 14) * 1024;
        bf16x8 A[4][4], B[4][4];
#define K2LD(bi, s)                                                                                  \
        do {                                                                                         \
            _Pragma("unroll") for (int m = 0; m < 4; ++m)                                            \
                A[bi][m] = __builtin_bit_cast(bf16x8, __builtin_amdgcn_raw_buffer_load_b128(ra, voff, abase + (m * 56 + (s)) * 1024, 0)); \
            _Pragma("unroll") for (int n = 0; n < 4; ++n)                                            \
                B[bi][n] = __builtin_bit_cast(bf16x8, __builtin_amdgcn_raw_buffer_load_b128(rbs, voff, bbase + (n * 56 + (s)) * 1024, 0)); \
        } while (0)
#define K2MM(bi)                                                                                     \
        do {                                                                                         \
            _Pragma("unroll") for (int m = 0; m < 4; ++m)                                            \
                _Pragma("unroll") for (int n = 0; n < 4; ++n)                                        \
                    acc[m][n] = __builtin_amdgcn_mfma_f32_16x16x32_bf16(A[bi][m], B[bi][n], acc[m][n], 0, 0, 0); \
        } while (0)
        K2LD(0, 0); K2LD(1, 1); K2LD(2, 2);
        SB;
#pragma unroll
        for (int s = 0; s < 14; ++s) {
            SB;
            if (s + 3 < 14) K2LD((s + 3) % 4, s + 3);
            SB;
            K2MM(s % 4);
        }
        SB;
    } else {
        const int wz = widu - 4;
        __builtin_amdgcn_s_setprio(2);
        const __amdgpu_buffer_rsrc_t ra = __builtin_amdgcn_make_buffer_rsrc((void*)a.ha, 0, 128 * 32 * 1024, 0x00020000);
        const __amdgpu_buffer_rsrc_t rbs = __builtin_amdgcn_make_buffer_rsrc((void*)a.cf, 0, 32 * 32 * 1024, 0x00020000);
        const int abase = (rb * 4 * 32 + wz * 4) * 1024, bbase = (cbk * 4 * 32 + wz * 4) * 1024;
        bf16x8 Hr[4], Hi[4], Br[2][4], Bi[2][4];
#define HLD(sl)                                                                                      \
        do {                                                                                         \
            _Pragma("unroll") for (int m = 0; m < 4; ++m) {                                          \
                Hr[m] = __builtin_bit_cast(bf16x8, __builtin_amdgcn_raw_buffer_load_b128(ra, voff, abase + (m * 32 + (sl)) * 1024, 0)); \
                Hi[m] = __builtin_bit_cast(bf16x8, __builtin_amdgcn_raw_buffer_load_b128(ra, voff, abase + (m * 32 + 16 + (sl)) * 1024, 0)); \
            }                                                                                        \
        } while (0)
#define ZLD(bi, sl)                                                                                  \
        do {                                                                                         \
            _Pragma("unroll") for (int n = 0; n < 4; ++n) {                                          \
                Br[bi][n] = __builtin_bit_cast(bf16x8, __builtin_amdgcn_raw_buffer_load_b128(rbs, voff, bbase + (n * 32 + (sl)) * 1024, 0)); \
                Bi[bi][n] = __builtin_bit_cast(bf16x8, __builtin_amdgcn_raw_buffer_load_b128(rbs, voff, bbase + (n * 32 + 16 + (sl)) * 1024, 0)); \
            }                                                                                        \
        } while (0)
        char* tab = smem + wz * SSM_TAB;
        {
            const int c0 = 128 * wz + 2 * lane;
            f32x4 ev[15];
            const f32x4* ep = (const f32x4*)(a.e256 + c0);
#pragma unroll
            for (int i = 0; i < 15; ++i) {
                int j = (rb >> 1) - 1 - i; j = j < -1 ? -1 : j;
                ev[i] = ep[j * (P_ / 2)];
            }
            const f32x4 pfx = *(const f32x4*)(a.e64 + (size_t)rb * P_ + c0);
            const f32x4 e32v = *(const f32x4*)(a.e32 + (size_t)rb * P_ + c0);
            const f32x4 l1 = *(const f32x4*)(a.lam + c0);
            const f32x4 l4 = *(const f32x4*)(a.lam + 512 + c0);
            const f32x4 l32 = *(const f32x4*)(a.lam + 1024 + c0);
            const f32x4 l64 = *(const f32x4*)(a.lam + 1536 + c0);
            const f32x4 l128 = *(const f32x4*)(a.lam + 2048 + c0);
            SB;
            HLD(0); ZLD(0, 0); ZLD(1, 1);
            SB;
            float Cr[2] = {0.f, 0.f}, Ci[2] = {0.f, 0.f};
#pragma unroll
            for (int i = 14; i >= 0; --i)
#pragma unroll
                for (int e = 0; e < 2; ++e) {
                    float nr = l128[2 * e] * Cr[e] - l128[2 * e + 1] * Ci[e] + ev[i][2 * e];
                    float ni = l128[2 * e] * Ci[e] + l128[2 * e + 1] * Cr[e] + ev[i][2 * e + 1];
                    Cr[e] = nr; Ci[e] = ni;
                }
            float c1r[2], c1i[2];
#pragma unroll
            for (int e = 0; e < 2; ++e) {
                float nr = l64[2 * e] * Cr[e] - l64[2 * e + 1] * Ci[e];
                float ni = l64[2 * e] * Ci[e] + l64[2 * e + 1] * Cr[e];
                Cr[e] = ((rb & 1) ? nr : Cr[e]) + pfx[2 * e];
                Ci[e] = ((rb & 1) ? ni : Ci[e]) + pfx[2 * e + 1];
                c1r[e] = e32v[2 * e] + l32[2 * e] * Cr[e] - l32[2 * e + 1] * Ci[e];
                c1i[e] = e32v[2 * e + 1] + l32[2 * e] * Ci[e] + l32[2 * e + 1] * Cr[e];
            }
#pragma unroll
            for (int sub = 0; sub < 2; ++sub) {
                float zr[2], zi[2];
#pragma unroll
                for (int e = 0; e < 2; ++e) {
                    float cr_ = sub ? c1r[e] : Cr[e], ci_ = sub ? c1i[e] : Ci[e];
                    zr[e] = cr_ * l1[2 * e] - ci_ * l1[2 * e + 1];
                    zi[e] = cr_ * l1[2 * e + 1] + ci_ * l1[2 * e];
                }
#pragma unroll
                for (int ii = 0; ii < 8; ++ii) {
                    *(f32x4*)(tab + (sub * 8 + ii) * ZS_STRIDE + lane * 16) = (f32x4){zr[0], zi[0], zr[1], zi[1]};
#pragma unroll
                    for (int e = 0; e < 2; ++e) {
                        float nr = zr[e] * l4[2 * e] - zi[e] * l4[2 * e + 1];
                        float ni = zr[e] * l4[2 * e + 1] + zi[e] * l4[2 * e];
                        zr[e] = nr; zi[e] = ni;
                    }
                }
            }
            *(f32x4*)(tab + LAM_OFF + lane * 16) = l1;
        }
        __builtin_amdgcn_s_waitcnt(0xC07F);
        __builtin_amdgcn_wave_barrier();
        SB;
        const char* zt = tab + i16 * ZS_STRIDE + (8 * g4) * 8;
        const char* lt = tab + LAM_OFF + (8 * g4) * 8;
#pragma unroll
        for (int sl = 0; sl < 4; ++sl) {
            const int cur = sl & 1;
            SB;
#pragma unroll
            for (int m = 0; m < 4; ++m)
#pragma unroll
                for (int n = 0; n < 4; ++n) {
                    acc[m][n] = __builtin_amdgcn_mfma_f32_16x16x32_bf16(Hr[m], Br[cur][n], acc[m][n], 0, 0, 0);
                    acc[m][n] = __builtin_amdgcn_mfma_f32_16x16x32_bf16(Hi[m], Bi[cur][n], acc[m][n], 0, 0, 0);
                }
            SB;
            if (sl + 1 < 4) HLD(sl + 1);
            SB;
            f32x4 z[4], lm[4];
#pragma unroll
            for (int q = 0; q < 4; ++q) {
                z[q] = *(const f32x4*)(zt + sl * 256 + q * 16);
                lm[q] = *(const f32x4*)(lt + sl * 256 + q * 16);
            }
#pragma unroll
            for (int m = 0; m < 4; ++m) {
                u32x4 zr, zi;
#pragma unroll
                for (int q = 0; q < 4; ++q) {
                    zr[q] = pack2(z[q][0], z[q][2]);
                    zi[q] = pack2(-z[q][1], -z[q][3]);
                    if (m < 3) {
                        float nr0 = z[q][0] * lm[q][0] - z[q][1] * lm[q][1];
                        float ni0 = z[q][0] * lm[q][1] + z[q][1] * lm[q][0];
                        float nr1 = z[q][2] * lm[q][2] - z[q][3] * lm[q][3];
                        float ni1 = z[q][2] * lm[q][3] + z[q][3] * lm[q][2];
                        z[q] = (f32x4){nr0, ni0, nr1, ni1};
                    }
                }
                bf16x8 Zr = __builtin_bit_cast(bf16x8, zr), Zi = __builtin_bit_cast(bf16x8, zi);
#pragma unroll
                for (int n = 0; n < 4; ++n) {
                    acc[m][n] = __builtin_amdgcn_mfma_f32_16x16x32_bf16(Zr, Br[cur][n], acc[m][n], 0, 0, 0);
                    acc[m][n] = __builtin_amdgcn_mfma_f32_16x16x32_bf16(Zi, Bi[cur][n], acc[m][n], 0, 0, 0);
                }
            }
            SB;
            if (sl + 2 < 4) ZLD(cur, sl + 2);
        }
        SB;
    }
    float gv[2][4], xv[2][4], dsk[2], bb[2];
#pragma unroll
    for (int f = 0; f < 2; ++f) {
        const int fr = wid * 2 + f;
        const int m = fr >> 2, n = fr & 3;
        const int d = col0 + 16 * n + i16;
        dsk[f] = b2f(a.Dsk[d]); bb[f] = b2f(a.b2[d]);
#pragma unroll
        for (int r = 0; r < 4; ++r) {
            const int t = row0 + 16 * g4 + 4 * r + m;
            gv[f][r] = a.gate[t];
            xv[f][r] = b2f(a.x[(size_t)t * D_ + d]);
        }
    }
    __syncthreads();
#pragma unroll
    for (int m = 0; m < 4; ++m)
#pragma unroll
        for (int n = 0; n < 4; ++n)
            *(f32x4*)(smem + ((wid * 16 + m * 4 + n) * 64 + lane) * 16) = acc[m][n];
    __syncthreads();
#pragma unroll
    for (int f = 0; f < 2; ++f) {
        const int fr = wid * 2 + f;
        const int m = fr >> 2, n = fr & 3;
        f32x4 sm_ = (f32x4){0.f, 0.f, 0.f, 0.f}, ss = (f32x4){0.f, 0.f, 0.f, 0.f};
#pragma unroll
        for (int w = 0; w < 4; ++w) {
            sm_ += *(const f32x4*)(smem + ((w * 16 + fr) * 64 + lane) * 16);
            ss += *(const f32x4*)(smem + (((w + 4) * 16 + fr) * 64 + lane) * 16);
        }
        const int d = col0 + 16 * n + i16;
#pragma unroll
        for (int r = 0; r < 4; ++r) {
            const int t = row0 + 16 * g4 + 4 * r + m;
            const float g = gv[f][r];
            const float av = 2.0f * ss[r] + rbf(xv[f][r] * dsk[f]);
            const float bv = rbf(rbf(sm_[r]) + bb[f]);
            a.out[(size_t)t * D_ + d] = g * av + rbf(rbf(1.0f - g) * bv);
        }
    }
}

extern "C" void kernel_launch(void* const* d_in, const int* in_sizes, int n_in,
                              void* d_out, int out_size, void* d_ws, size_t ws_size,
                              hipStream_t stream) {
    char* ws = (char*)d_ws;
    K1Args a1;
    a1.x = (const bf16*)d_in[0]; a1.lre = (const bf16*)d_in[1]; a1.lim = (const bf16*)d_in[2];
    a1.Bre = (const bf16*)d_in[3]; a1.Bim = (const bf16*)d_in[4]; a1.logdt = (const bf16*)d_in[7];
    a1.w1 = (const bf16*)d_in[10]; a1.b1 = (const bf16*)d_in[11]; a1.w2 = (const bf16*)d_in[12];
    a1.gw = (const bf16*)d_in[14];
    a1.Cre = (const bf16*)d_in[5]; a1.Cim = (const bf16*)d_in[6]; a1.cf = (bf16*)(ws + WS_CF);
    a1.ha = (bf16*)(ws + WS_HA); a1.hm = (bf16*)(ws + WS_HM); a1.w2t = (bf16*)(ws + WS_W2T);
    a1.e64 = (float2*)(ws + WS_E64); a1.e32 = (float2*)(ws + WS_E32); a1.gate = (float*)(ws + WS_G);
    a1.lam = (float2*)(ws + WS_LAM); a1.e256 = (float2*)(ws + WS_E256);
    K2Args a2;
    a2.x = a1.x;
    a2.cf = a1.cf; a2.Dsk = (const bf16*)d_in[8];
    a2.b2 = (const bf16*)d_in[13];
    a2.ha = a1.ha; a2.hm = a1.hm; a2.w2t = a1.w2t; a2.e64 = a1.e64; a2.e32 = a1.e32; a2.gate = a1.gate;
    a2.lam = a1.lam; a2.e256 = a1.e256;
    a2.out = (float*)d_out;
    k1_kernel<<<240, 512, 0, stream>>>(a1);
    k2_kernel<<<256, 512, 0, stream>>>(a2);
}
```
